```python
import torch

def _softmax(x, axis=-1):
    return torch.softmax(x, dim=axis)
def _gelu(x):
    return torch.nn.functional.gelu(x, approximate="tanh")
def _silu(x):
    return torch.nn.functional.silu(x)
def _sigmoid(x):
    return torch.sigmoid(x)
def _layernorm(x, axis=-1, eps=1e-5):
    m = x.mean(dim=axis, keepdim=True)
    v = x.var(dim=axis, keepdim=True, unbiased=False)
    return (x - m) / torch.sqrt(v + eps)
def _scatter_add(tgt, idx, src, axis=0):
    return tgt.clone().index_add_(axis, idx, src)
def _segment_sum(src, seg, n):
    out = torch.zeros((n,) + src.shape[1:], dtype=src.dtype)
    return out.index_add_(0, seg, src)
def _pad(x, pw):
    flat = tuple(v for pair in reversed(pw) for v in pair)
    return torch.nn.functional.pad(x, flat)

kernel_name = 'gather_scatter__cheap_equivariant_egnn__graph'

def setup_inputs():

    _w = (lambda a: (lambda t: t.to(torch.bfloat16) if t.is_floating_point() else t)(torch.as_tensor(a)))
    N, deg, D, De = 50000, 16, 64, max(0, 1)
    E = N * deg
    return {
        "h": _w((lambda *s: torch.randn(*s, dtype=torch.bfloat16) * 0.2)(N, D)),
        "pos": _w((lambda *s: torch.randn(*s, dtype=torch.bfloat16) * 0.4)(N, 3)),
        "e_attr": _w((lambda *s: torch.randn(*s, dtype=torch.bfloat16) * 0.4)(E, De)),
        "src": _w((lambda lo, hi, *s: torch.randint(lo, hi, s))(0, N, E).to(torch.int64)),
        "dst": _w((lambda lo, hi, *s: torch.randint(lo, hi, s))(0, N, E).to(torch.int64)),
        "w_e": _w(0.1 * (lambda *s: torch.randn(*s, dtype=torch.bfloat16) * 0.4)(2 * D + 1 + De, D)),
        "w_x": _w(0.1 * (lambda *s: torch.randn(*s, dtype=torch.bfloat16) * 0.2)(D, 1)),
        "w_h": _w(0.1 * (lambda *s: torch.randn(*s, dtype=torch.bfloat16) * 0.2)(2 * D, D)),
    }

def reference(h, pos, e_attr, src, dst, w_e, w_x, w_h):
    N, D = h.shape[0], h.shape[1]
    diff = pos[dst] - pos[src]
    dist2 = torch.sum(diff * diff, axis=-1, keepdims=True)
    e_in = torch.concatenate([h[dst], h[src], dist2, e_attr], axis=-1)
    m_ij = _silu(e_in @ w_e)
    coord_scale = m_ij @ w_x
    pos_new = pos + _segment_sum(diff * coord_scale, dst, N)
    m_i = _segment_sum(m_ij, dst, N)
    h_new = h + _silu(torch.concatenate([h, m_i], axis=-1) @ w_h)
    return h_new, pos_new
```

```cpp
#include <hip/hip_runtime.h>
#include <hip/hip_bf16.h>
#include <stdint.h>

static __device__ __forceinline__ float bf2f(unsigned short v) { return __uint_as_float(((unsigned)v) << 16); }
static __device__ __forceinline__ unsigned short f2bf(float f) { __hip_bfloat16 b = __float2bfloat16(f); return *(unsigned short*)&b; }

__global__ void zero_k(float* p, int n) { int i = blockIdx.x * blockDim.x + threadIdx.x; if (i < n) p[i] = 0.f; }

__global__ void edge_k(const unsigned short* h, const unsigned short* pos, const unsigned short* ea, const int* src, const int* dst,
                       const unsigned short* we, const unsigned short* wx, float* mi, float* pd, int E) {
  __shared__ float sw[130 * 64];
  for (int i = threadIdx.x; i < 130 * 64; i += blockDim.x) sw[i] = bf2f(we[i]);
  __syncthreads();
  int lane = threadIdx.x & 63, w = threadIdx.x >> 6;
  int nw = blockDim.x >> 6;
  float wxf = bf2f(wx[lane]);
  for (int e = blockIdx.x * nw + w; e < E; e += gridDim.x * nw) {
    int s = src[e], d = dst[e];
    float df[3]; float d2 = 0.f;
    for (int c = 0; c < 3; ++c) { df[c] = bf2f(f2bf(bf2f(pos[d * 3 + c]) - bf2f(pos[s * 3 + c]))); d2 += bf2f(f2bf(df[c] * df[c])); }
    d2 = bf2f(f2bf(d2));
    float hd = bf2f(h[d * 64 + lane]), hs = bf2f(h[s * 64 + lane]);
    float acc = 0.f;
    _Pragma("unroll 4") for (int k = 0; k < 64; ++k) acc += __shfl(hd, k) * sw[k * 64 + lane];
    _Pragma("unroll 4") for (int k = 0; k < 64; ++k) acc += __shfl(hs, k) * sw[(64 + k) * 64 + lane];
    acc += d2 * sw[128 * 64 + lane] + bf2f(ea[e]) * sw[129 * 64 + lane];
    float m = acc / (1.f + __expf(-acc));
    m = bf2f(f2bf(m));
    float cs = m * wxf;
    for (int o = 32; o > 0; o >>= 1) cs += __shfl_xor(cs, o);
    atomicAdd(&mi[d * 64 + lane], m);
    if (lane < 3) atomicAdd(&pd[d * 3 + lane], df[lane] * cs);
  }
}

__global__ void node_k(const unsigned short* h, const unsigned short* pos, const unsigned short* wh, const float* mi, const float* pd, unsigned short* out, int N) {
  __shared__ float sw[128 * 64];
  for (int i = threadIdx.x; i < 128 * 64; i += blockDim.x) sw[i] = bf2f(wh[i]);
  __syncthreads();
  int lane = threadIdx.x & 63, w = threadIdx.x >> 6;
  int nw = blockDim.x >> 6;
  for (int n = blockIdx.x * nw + w; n < N; n += gridDim.x * nw) {
    float hv = bf2f(h[n * 64 + lane]); float mv = bf2f(f2bf(mi[n * 64 + lane]));
    float acc = 0.f;
    _Pragma("unroll 4") for (int k = 0; k < 64; ++k) acc += __shfl(hv, k) * sw[k * 64 + lane];
    _Pragma("unroll 4") for (int k = 0; k < 64; ++k) acc += __shfl(mv, k) * sw[(64 + k) * 64 + lane];
    float s = acc / (1.f + __expf(-acc));
    out[n * 64 + lane] = f2bf(hv + s);
    if (lane < 3) out[N * 64 + n * 3 + lane] = f2bf(bf2f(pos[n * 3 + lane]) + pd[n * 3 + lane]);
  }
}

extern "C" void kernel_launch(void* const* d_in, const int* in_sizes, int n_in, void* d_out, int out_size, void* d_ws, size_t ws_size, hipStream_t stream) {
  const unsigned short* h = (const unsigned short*)d_in[0];
  const unsigned short* pos = (const unsigned short*)d_in[1];
  const unsigned short* ea = (const unsigned short*)d_in[2];
  const int* src = (const int*)d_in[3];
  const int* dst = (const int*)d_in[4];
  const unsigned short* we = (const unsigned short*)d_in[5];
  const unsigned short* wx = (const unsigned short*)d_in[6];
  const unsigned short* wh = (const unsigned short*)d_in[7];
  int N = in_sizes[0] / 64, E = in_sizes[3];
  float* mi = (float*)d_ws; float* pd = mi + (size_t)N * 64;
  int nz = N * 67;
  zero_k<<<(nz + 255) / 256, 256, 0, stream>>>(mi, nz);
  edge_k<<<2048, 256, 0, stream>>>(h, pos, ea, src, dst, we, wx, mi, pd, E);
  node_k<<<1024, 256, 0, stream>>>(h, pos, wh, mi, pd, (unsigned short*)d_out, N);
}
```

```cpp
#include <hip/hip_runtime.h>
#include <hip/hip_bf16.h>
#include <stdint.h>

#define FOLD 1

typedef unsigned short u16;
typedef unsigned u32;
typedef short bf16x8 __attribute__((ext_vector_type(8)));
typedef short s16x4 __attribute__((ext_vector_type(4)));
typedef float f32x16 __attribute__((ext_vector_type(16)));
typedef float f32x4 __attribute__((ext_vector_type(4)));
typedef unsigned u32x4 __attribute__((ext_vector_type(4)));
typedef unsigned u32x2 __attribute__((ext_vector_type(2)));
#define LAS __attribute__((address_space(3)))

constexpr int NPB = 196;
constexpr int NBK = 256;
constexpr int NCH = 256;
constexpr int MAXT = 112;
constexpr int OVCAP = 64;
constexpr float LOG2E = 1.4426950408889634f, LN2 = 0.6931471805599453f;

constexpr int L_STG = 0;
constexpr int L_V = 65536;
constexpr int L_DE = L_V + MAXT * 32 * 8;
constexpr int L_SR = L_DE + MAXT * 32 * 4;
constexpr int L_DS = L_SR + MAXT * 32 * 2;
constexpr int L_HM = L_DS + MAXT * 32;
constexpr int L_PT = L_HM + 197 * 144;
constexpr int L_PA = L_PT + NPB * 16;
constexpr int L_CNT = L_PA + NPB * 16;
constexpr int L_OFF = L_CNT + 1024;
constexpr int L_WX = L_OFF + 1024;
constexpr int L_Z = L_WX + 256;
constexpr int L_MISC = L_Z + 256;
constexpr int L_OV = L_MISC + 64;
constexpr int L_END = L_OV + OVCAP * 16;
static_assert(L_HM % 16 == 0 && L_PT % 16 == 0 && L_CNT % 16 == 0 && L_Z % 16 == 0 && L_OV % 16 == 0, "align");
static_assert(L_END <= 160 * 1024, "lds");

constexpr size_t W_TAB = 0;
constexpr size_t W_WE = W_TAB + NCH * NBK * 4;
constexpr size_t W_WH = W_WE + 18 * 1024;
constexpr size_t W_REC = W_WH + 16 * 1024;

static __device__ __forceinline__ float bf2f(u32 v) { return __uint_as_float(v << 16); }
static __device__ __forceinline__ u32 cvtpk(float lo, float hi) { u32 r; asm("v_cvt_pk_bf16_f32 %0, %1, %2" : "=v"(r) : "v"(lo), "v"(hi)); return r; }
static __device__ __forceinline__ float lo2f(u32 v) { return __uint_as_float(v << 16); }
static __device__ __forceinline__ float hi2f(u32 v) { return __uint_as_float(v & 0xffff0000u); }
template <int CTRL, int RM = 0xf> static __device__ __forceinline__ u32 dpp0(u32 x) { return (u32)__builtin_amdgcn_update_dpp(0, (int)x, CTRL, RM, 0xf, true); }
static __device__ __forceinline__ u32 wave_incl_scan(u32 x) {
  x += dpp0<0x111>(x); x += dpp0<0x112>(x); x += dpp0<0x114>(x); x += dpp0<0x118>(x);
  x += dpp0<0x142, 0xa>(x); x += dpp0<0x143, 0xc>(x); return x; }
#define MFMA(a, b, c) __builtin_amdgcn_mfma_f32_32x32x16_bf16(a, b, c, 0, 0, 0)
#define LBAR() asm volatile("s_waitcnt lgkmcnt(0)\n\ts_barrier" ::: "memory")
template <class T> static __device__ __forceinline__ T ldl(u32 a) { return *(LAS const T*)(size_t)a; }
template <class T> static __device__ __forceinline__ void stl(u32 a, T v) { *(LAS T*)(size_t)a = v; }
static __device__ __forceinline__ s16x4 ldtr(u32 a) { typedef short v4 __attribute__((ext_vector_type(4)));
  return __builtin_bit_cast(s16x4, __builtin_amdgcn_ds_read_tr16_b64_v4i16((LAS v4*)(size_t)a)); }
static __device__ __forceinline__ float silu_f(float z) { return z * __builtin_amdgcn_rcpf(1.f + __builtin_amdgcn_exp2f(-LOG2E * z)); }

__global__ void __launch_bounds__(1024) k1_partition(const int* __restrict__ dst, const int* __restrict__ src, const u16* __restrict__ ea,
                                                    const u16* __restrict__ we, const u16* __restrict__ wh, char* __restrict__ ws, int E, int CS) {
  __shared__ u32x2 srec[4096];
  __shared__ __attribute__((aligned(16))) u32 cnt[NBK];
  __shared__ __attribute__((aligned(16))) u32 start[NBK];
  const int tid = threadIdx.x, c = blockIdx.x;
  if (c == NCH) {
    for (int i = tid; i < 34 * 64; i += 1024) {
      const int fr = i >> 6, l = i & 63, isH = fr >= 18, g = isH ? fr - 18 : fr, s = g >> 1, ft = g & 1;
      const u16* w = isH ? wh : we; const int kmax = isH ? 128 : 130;
      float v[8];
#pragma unroll
      for (int j = 0; j < 8; ++j) { const int k = 16 * s + 8 * (l >> 5) + j; v[j] = k < kmax ? bf2f(w[k * 64 + 32 * ft + (l & 31)]) : 0.f; if (FOLD && !isH) v[j] *= -LOG2E; }
      u32x4 o; o.x = cvtpk(v[0], v[1]); o.y = cvtpk(v[2], v[3]); o.z = cvtpk(v[4], v[5]); o.w = cvtpk(v[6], v[7]);
      *(u32x4*)(ws + W_WE + (size_t)i * 16) = o;
    }
    return;
  }
  const int e0 = c * CS + 4 * tid;
  const bool act = 4 * tid < CS && e0 < E;
  u32x4 d4 = {0, 0, 0, 0}, s4 = {0, 0, 0, 0}; u32x2 a4 = {0, 0};
  if (act) { d4 = *(const u32x4*)(dst + e0); s4 = *(const u32x4*)(src + e0); a4 = *(const u32x2*)(ea + e0); }
  if (tid < NBK) cnt[tid] = 0;
  __syncthreads();
  u32 b[4], r[4];
  if (act) {
#pragma unroll
    for (int j = 0; j < 4; ++j) { b[j] = d4[j] / (u32)NPB; r[j] = atomicAdd(&cnt[b[j]], 1u); }
  }
  __syncthreads();
  if (tid < 64) {
    const u32x4 cv = *(const u32x4*)&cnt[4 * tid];
    const u32 s1 = cv.x + cv.y, s2 = s1 + cv.z, s3 = s2 + cv.w;
    const u32 ex = wave_incl_scan(s3) - s3;
    u32x4 st = {ex, ex + cv.x, ex + s1, ex + s2};
    *(u32x4*)&start[4 * tid] = st;
    u32x4 t = {st.x | cv.x << 16, st.y | cv.y << 16, st.z | cv.z << 16, st.w | cv.w << 16};
    *(u32x4*)(ws + W_TAB + ((size_t)c * NBK + 4 * tid) * 4) = t;
  }
  __syncthreads();
  if (act) {
#pragma unroll
    for (int j = 0; j < 4; ++j) {
      const u32 eav = (j & 1) ? (a4[j >> 1] & 0xffff0000u) : (a4[j >> 1] << 16);
      u32x2 rec = {s4[j] | (d4[j] - b[j] * NPB) << 16, eav};
      srec[start[b[j]] + r[j]] = rec;
    }
  }
  __syncthreads();
  const int n = min(CS, E - c * CS);
  u32x4* o = (u32x4*)(ws + W_REC + (size_t)c * CS * 8);
  for (int i = tid; 2 * i < n; i += 1024) o[i] = *(const u32x4*)&srec[2 * i];
}

struct Dma { const char* b0; const char* b1; const char* b2; const char* b3; };
static __device__ __forceinline__ void dma4(u32 v0, u32 v1, u32 v2, u32 v3, const Dma& d, u32 lds) {
  u32 sv;
  asm volatile("s_mov_b32 %0, m0\n\ts_mov_b32 m0, %9\n\ts_nop 0\n\t"
               "global_load_lds_dwordx4 %1, %5\n\t"
               "global_load_lds_dwordx4 %2, %6 offset:1024\n\t"
               "global_load_lds_dwordx4 %3, %7 offset:2048\n\t"
               "global_load_lds_dwordx4 %4, %8 offset:3072\n\t"
               "s_mov_b32 m0, %0"
               : "=&s"(sv) : "v"(v0), "v"(v1), "v"(v2), "v"(v3), "s"(d.b0), "s"(d.b1), "s"(d.b2), "s"(d.b3), "s"(lds) : "memory");
}

static __device__ __forceinline__ void slow_bucket(const u16* __restrict__ h, const u16* __restrict__ pos, const u16* __restrict__ we, const u16* __restrict__ wx,
                                                const u16* __restrict__ wh, const char* __restrict__ ws, u16* __restrict__ out, int N, int CS, int bk, u32 l0) {
  const int tid = threadIdx.x, lane = tid & 63, w = tid >> 6, n0 = bk * NPB;
  LAS float* macc = (LAS float*)(size_t)(l0 + L_STG);
  LAS float* pacc = (LAS float*)(size_t)(l0 + L_PA);
  __syncthreads();
  for (int i = tid; i < NPB * 64; i += 512) macc[i] = 0.f;
  for (int i = tid; i < NPB * 4; i += 512) pacc[i] = 0.f;
  __syncthreads();
  const float wxf = bf2f(wx[lane]);
  for (int c = w; c < NCH; c += 8) {
    const u32 ent = *(const u32*)(ws + W_TAB + ((size_t)c * NBK + bk) * 4);
    const int st = ent & 0xffff, cn = ent >> 16;
    for (int j = 0; j < cn; ++j) {
      const u32x2 rec = *(const u32x2*)(ws + W_REC + ((size_t)c * CS + st + j) * 8);
      const int s = rec.x & 0xffff, dl = rec.x >> 16, d = n0 + dl;
      float df[3], d2 = 0.f;
#pragma unroll
      for (int k = 0; k < 3; ++k) { df[k] = lo2f(cvtpk(bf2f(pos[d * 3 + k]) - bf2f(pos[s * 3 + k]), 0.f)); d2 += df[k] * df[k]; }
      d2 = lo2f(cvtpk(d2, 0.f));
      const float hd = bf2f(h[(size_t)d * 64 + lane]), hs = bf2f(h[(size_t)s * 64 + lane]);
      float z = 0.f;
#pragma unroll 4
      for (int k = 0; k < 64; ++k) z += __shfl(hd, k) * bf2f(we[k * 64 + lane]) + __shfl(hs, k) * bf2f(we[(64 + k) * 64 + lane]);
      z += d2 * bf2f(we[128 * 64 + lane]) + hi2f(rec.y) * bf2f(we[129 * 64 + lane]);
      const float m = lo2f(cvtpk(silu_f(z), 0.f));
      float cs = m * wxf;
#pragma unroll
      for (int o = 32; o > 0; o >>= 1) cs += __shfl_xor(cs, o);
      atomicAdd((float*)&macc[dl * 64 + lane], m);
      if (lane < 3) atomicAdd((float*)&pacc[dl * 4 + lane], (lane == 0 ? df[0] : lane == 1 ? df[1] : df[2]) * cs);
    }
  }
  __syncthreads();
  for (int nl = w; nl < NPB; nl += 8) {
    const int n = n0 + nl; if (n >= N) break;
    const float hv = bf2f(h[(size_t)n * 64 + lane]), mv = lo2f(cvtpk(macc[nl * 64 + lane], 0.f));
    float z = 0.f;
#pragma unroll 4
    for (int k = 0; k < 64; ++k) z += __shfl(hv, k) * bf2f(wh[k * 64 + lane]) + __shfl(mv, k) * bf2f(wh[(64 + k) * 64 + lane]);
    out[(size_t)n * 64 + lane] = (u16)cvtpk(hv + silu_f(z), 0.f);
    if (lane < 3) out[(size_t)N * 64 + (size_t)n * 3 + lane] = (u16)cvtpk(bf2f(pos[n * 3 + lane]) + pacc[nl * 4 + lane], 0.f);
  }
}

__global__ void __launch_bounds__(512, 2) k2_main(const u16* __restrict__ h, const u16* __restrict__ pos, const u16* __restrict__ we, const u16* __restrict__ wx,
                                                  const u16* __restrict__ wh, const char* __restrict__ ws, u16* __restrict__ out, int N, int CS) {
  extern __shared__ __attribute__((aligned(16))) char smem[];
  const u32 l0 = (u32)(size_t)(LAS char*)smem;
  const int tid = threadIdx.x, lane = tid & 63;
  const int w = __builtin_amdgcn_readfirstlane(tid >> 6);
  const int bk = (blockIdx.x & 7) * 32 + (blockIdx.x >> 3);
  const int n0 = bk * NPB;
  if (n0 >= N) return;

  const int grp = lane >> 4, j16 = lane & 15;
  const int chunk0 = 32 * w + 8 * grp;
  const u32 ent = *(const u32*)(ws + W_TAB + ((size_t)(chunk0 + (j16 & 7)) * NBK + bk) * 4);
  u32x4 ht[4], wt[3];
#pragma unroll
  for (int i = 0; i < 4; ++i) { const int pc = tid + 512 * i, row = pc >> 3; ht[i] = u32x4{0, 0, 0, 0};
    if (row < NPB && n0 + row < N) ht[i] = *(const u32x4*)(h + (size_t)(n0 + row) * 64 + (pc & 7) * 8); }
#pragma unroll
  for (int i = 0; i < 3; ++i) { const int pc = tid + 512 * i; wt[i] = u32x4{0, 0, 0, 0}; if (pc < 18 * 64) wt[i] = *(const u32x4*)(ws + W_WE + (size_t)pc * 16); }
  float px = 0.f, py = 0.f, pz = 0.f;
  if (tid < NPB && n0 + tid < N) { const u16* p = pos + (size_t)(n0 + tid) * 3; px = bf2f(p[0]); py = bf2f(p[1]); pz = bf2f(p[2]); }
  float wxv = 0.f; if (tid < 64) wxv = bf2f(wx[tid]) * (FOLD ? -LN2 : 1.f);
  if (tid < 256) stl<u32>(l0 + L_CNT + 4 * tid, 0u);
  if (tid < 16) stl<u32>(l0 + L_MISC + 4 * tid, tid < 8 ? 0xffffffffu : 0u);
  if (tid < 64) stl<u32>(l0 + L_Z + 4 * tid, 0u);
  if (tid < NPB) stl<f32x4>(l0 + L_PA + 16 * tid, f32x4{0, 0, 0, 0});
  LBAR();

  u32x2 rec[16]; u32 vmask = 0;
  u32 segb[8], segc[8];
#define SEG_ENT(i) { const u32 e_ = dpp0<0x150 + i>(ent); segc[i] = e_ >> 16; segb[i] = (u32)(chunk0 + i) * (u32)CS + (e_ & 0xffff) + j16; }
  SEG_ENT(0) SEG_ENT(1) SEG_ENT(2) SEG_ENT(3) SEG_ENT(4) SEG_ENT(5) SEG_ENT(6) SEG_ENT(7)
  const u32x2* recs = (const u32x2*)(ws + W_REC);
#pragma unroll
  for (int i = 0; i < 16; ++i) { const int s = i & 7, r = i >> 3; rec[i] = u32x2{0, 0};
    if ((u32)(16 * r + j16) < segc[s]) { rec[i] = recs[segb[s] + 16 * r]; vmask |= 1u << i; } }
#pragma unroll
  for (int i = 0; i < 4; ++i) { const int pc = tid + 512 * i, row = pc >> 3; if (row <= NPB) stl<u32x4>(l0 + L_HM + row * 144 + (pc & 7) * 16, ht[i]); }
#pragma unroll
  for (int i = 0; i < 3; ++i) { const int pc = tid + 512 * i; if (pc < 18 * 64) stl<u32x4>(l0 + L_STG + pc * 16, wt[i]); }
  if (tid < NPB) stl<f32x4>(l0 + L_PT + 16 * tid, f32x4{px, py, pz, 0.f});
  if (tid < 64) stl<float>(l0 + L_WX + 4 * tid, wxv);
  u32 rank[16]; u32x2 ps[16];
  const char* posb = (const char*)pos;
#pragma unroll
  for (int i = 0; i < 16; ++i) { rank[i] = 0; ps[i] = u32x2{0, 0};
    if (vmask >> i & 1) {
      ps[i] = *(const u32x2*)(posb + (((rec[i].x & 0xffff) * 6u) & ~3u));
      rank[i] = __hip_atomic_fetch_add((LAS u32*)(size_t)(l0 + L_CNT + 4 * (rec[i].x >> 16)), 1u, __ATOMIC_RELAXED, __HIP_MEMORY_SCOPE_WORKGROUP);
    } }
  {
    u32 mx = 0;
#pragma unroll
    for (int s = 0; s < 8; ++s) mx = max(mx, segc[s]);
    if (__builtin_amdgcn_ballot_w64(mx > 32) != 0) {
#pragma unroll
      for (int s = 0; s < 8; ++s)
        for (u32 k = 32 + j16; __builtin_amdgcn_ballot_w64(k < segc[s]) != 0; k += 16)
          if (k < segc[s]) {
            const u32 slot = __hip_atomic_fetch_add((LAS u32*)(size_t)(l0 + L_MISC + 36), 1u, __ATOMIC_RELAXED, __HIP_MEMORY_SCOPE_WORKGROUP);
            if (slot < OVCAP) {
              const u32x2 rr = recs[segb[s] - j16 + k];
              const u32 rk = __hip_atomic_fetch_add((LAS u32*)(size_t)(l0 + L_CNT + 4 * (rr.x >> 16)), 1u, __ATOMIC_RELAXED, __HIP_MEMORY_SCOPE_WORKGROUP);
              stl<u32x4>(l0 + L_OV + 16 * slot, u32x4{rr.x, rr.y, rk, 0u});
            }
          }
    }
  }
  LBAR();
  const u32 novf = __builtin_amdgcn_readfirstlane(ldl<u32>(l0 + L_MISC + 36));
  bf16x8 W[18];
#pragma unroll
  for (int i = 0; i < 18; ++i) W[i] = ldl<bf16x8>(l0 + L_STG + i * 1024 + lane * 16);
  u32 wa0, wa1, wtb, wnt, wend; bool irregular;
  {
    const u32x4 cv = ldl<u32x4>(l0 + L_CNT + 16 * lane);
    const u32 s1 = cv.x + cv.y, s2 = s1 + cv.z, s3 = s2 + cv.w;
    const u32 inc = wave_incl_scan(s3), ex = inc - s3;
    const u32 T = __builtin_amdgcn_readlane(inc, 63);
    const u32 off[4] = {ex, ex + cv.x, ex + s1, ex + s2};
    const float rT = T ? 8.f / (float)T : 0.f;
    u32 wv[4];
#pragma unroll
    for (int i = 0; i < 4; ++i) wv[i] = min(7u, (u32)((float)off[i] * rT));
    u32 prev = (u32)__builtin_amdgcn_update_dpp(-1, (int)wv[3], 0x138, 0xf, 0xf, false);
#pragma unroll
    for (int i = 0; i < 4; ++i) { const u32 n = 4 * lane + i;
      if (n < NPB && wv[i] != prev) stl<u32>(l0 + L_MISC + 4 * wv[i], off[i] << 8 | n);
      prev = wv[i]; }
    u32 cut = min(ldl<u32>(l0 + L_MISC + 4 * (lane & 7)), T << 8 | NPB);
    if (j16 >= 8) cut = T << 8 | NPB;
    cut = min(cut, (u32)__builtin_amdgcn_update_dpp(-1, (int)cut, 0x101, 0xf, 0xf, false));
    cut = min(cut, (u32)__builtin_amdgcn_update_dpp(-1, (int)cut, 0x102, 0xf, 0xf, false));
    cut = min(cut, (u32)__builtin_amdgcn_update_dpp(-1, (int)cut, 0x104, 0xf, 0xf, false));
    const u32 nxt = (u32)__builtin_amdgcn_update_dpp(-1, (int)cut, 0x101, 0xf, 0xf, false);
    u32 len = j16 < 8 ? (nxt >> 8) - (cut >> 8) : 0, nt = (len + 31) >> 5;
    u32 tb = nt; tb += dpp0<0x111>(tb); tb += dpp0<0x112>(tb); tb += dpp0<0x114>(tb);
    const u32 ttot = __builtin_amdgcn_readlane(tb, 7);
    tb -= nt;
    const u32 delta = 32 * tb - (cut >> 8);
    u32x4 op;
#pragma unroll
    for (int i = 0; i < 4; ++i) op[i] = off[i] + (u32)__builtin_amdgcn_ds_bpermute(4 * wv[i], (int)delta);
    stl<u32x4>(l0 + L_OFF + 16 * lane, op);
    wa0 = __builtin_amdgcn_readlane(cut, w) & 0xff; wa1 = __builtin_amdgcn_readlane(nxt, w) & 0xff;
    wtb = __builtin_amdgcn_readlane(tb, w); wnt = __builtin_amdgcn_readlane(nt, w);
    wend = 32 * wtb + __builtin_amdgcn_readlane(len, w);
    irregular = ttot > MAXT || novf > OVCAP;
  }
  if (irregular) { slow_bucket(h, pos, we, wx, wh, ws, out, N, CS, bk, l0); return; }

  u32 pp[16];
#pragma unroll
  for (int i = 0; i < 16; ++i) { pp[i] = 0;
    if (vmask >> i & 1) {
      const u32 dl = rec[i].x >> 16, p = ldl<u32>(l0 + L_OFF + 4 * dl) + rank[i]; pp[i] = p;
      const u32 q = (p & ~31u) | (p & 7) << 2 | (p >> 3 & 3);
      stl<u16>(l0 + L_SR + 2 * q, (u16)rec[i].x); stl<unsigned char>(l0 + L_DS + q, (unsigned char)dl);
    } }
  auto edge_geom = [&](u32 rx, u32 ry, u32x2 pw, u32 p) {
    const u32 odd = rx & 1;
    const float sx = odd ? hi2f(pw.x) : lo2f(pw.x), sy = odd ? lo2f(pw.y) : hi2f(pw.x), sz = odd ? hi2f(pw.y) : lo2f(pw.y);
    const f32x4 pd = ldl<f32x4>(l0 + L_PT + 16 * (rx >> 16));
    const u32 v0 = cvtpk(1.f, pd.x - sx), v1 = cvtpk(pd.y - sy, pd.z - sz);
    const float dx = hi2f(v0), dy = lo2f(v1), dz = hi2f(v1);
    const float d2 = dx * dx + dy * dy + dz * dz;
    stl<u32x2>(l0 + L_V + 8 * p, u32x2{v0, v1});
    stl<u32>(l0 + L_DE + 4 * p, (cvtpk(d2, 0.f) & 0xffffu) | ry);
  };
  if (novf) {
    if ((u32)tid < novf) {
      const u32x4 o = ldl<u32x4>(l0 + L_OV + 16 * tid);
      const u32 dl = o.x >> 16, p = ldl<u32>(l0 + L_OFF + 4 * dl) + o.z;
      const u32 q = (p & ~31u) | (p & 7) << 2 | (p >> 3 & 3);
      stl<u16>(l0 + L_SR + 2 * q, (u16)o.x); stl<unsigned char>(l0 + L_DS + q, (unsigned char)dl);
      const u32x2 pw = *(const u32x2*)(posb + (((o.x & 0xffff) * 6u) & ~3u));
      edge_geom(o.x, o.y, pw, p);
    }
  }
  {
    const u32 p = wend + lane;
    if (p < 32 * (wtb + wnt)) {
      const u32 q = (p & ~31u) | (p & 7) << 2 | (p >> 3 & 3);
      stl<u16>(l0 + L_SR + 2 * q, (u16)0); stl<unsigned char>(l0 + L_DS + q, (unsigned char)NPB);
      stl<u32x2>(l0 + L_V + 8 * p, u32x2{0, 0}); stl<u32>(l0 + L_DE + 4 * p, 0u);
    }
  }
  LBAR();
  const Dma dh = {(const char*)h, (const char*)h - 1024, (const char*)h - 2048, (const char*)h - 3072};
  const u32 slot0 = l0 + L_STG + w * 8192;
  const u32 dch0 = 16 * ((lane & 7) ^ (lane >> 4)), dch1 = dch0 ^ 64;
  const u32 sraddr = l0 + L_SR + 64 * wtb + 8 * (lane >> 3);
  auto issue = [&](u32x2 sv, u32 slot) {
    dma4((sv.x & 0xffff) * 128 + dch0, (sv.x >> 16) * 128 + dch1, (sv.y & 0xffff) * 128 + dch0, (sv.y >> 16) * 128 + dch1, dh, slot);
  };
  if (wnt > 0) issue(ldl<u32x2>(sraddr), slot0);
  if (wnt > 1) issue(ldl<u32x2>(sraddr + 64), slot0 + 4096);
#pragma unroll
  for (int i = 0; i < 16; ++i) if (vmask >> i & 1) edge_geom(rec[i].x, rec[i].y, ps[i], pp[i]);
  LBAR();

  const int e32 = lane & 31, hh = lane >> 5;
  const u32 swz = (e32 >> 1) & 7;
  u32 sfa[4];
#pragma unroll
  for (int s = 0; s < 4; ++s) sfa[s] = e32 * 128 + 16 * ((2 * s + hh) ^ swz);
  const u32 eq = 4 * hh + ((lane >> 2) & 3), slotA = 4 * ((lane >> 4) & 1) + (lane & 3);
  const u32 zaddr = l0 + L_Z;
  u32 a_dsb = l0 + L_DS + 32 * wtb + 4 * (e32 & 7) + (e32 >> 3);
  u32 a_dsw = l0 + L_DS + 32 * wtb + 4 * eq;
  u32 a_de = l0 + L_DE + 128 * wtb + 4 * e32;
  u32 a_v = l0 + L_V + 256 * wtb + 8 * eq;
  u32 a_sr = sraddr + 128;
  const u32 hmb = l0 + L_HM + 16 * hh;
  f32x16 Y0, Y1;
#pragma unroll
  for (int r = 0; r < 16; ++r) { Y0[r] = 0.f; Y1[r] = 0.f; }
  u32 base = wa0;
  const u32 fn = (lane & 31) >> 2, fi = lane & 3;
  auto flush = [&]() {
    const u32 node = base + fn;
    float dot = 0.f;
#pragma unroll
    for (int q = 0; q < 4; ++q) {
      const f32x4 w0 = ldl<f32x4>(l0 + L_WX + 32 * q + 16 * hh), w1 = ldl<f32x4>(l0 + L_WX + 128 + 32 * q + 16 * hh);
#pragma unroll
      for (int k = 0; k < 4; ++k) { dot += Y0[4 * q + k] * w0[k]; dot += Y1[4 * q + k] * w1[k]; }
    }
    auto sw = __builtin_amdgcn_permlane32_swap(__float_as_uint(dot), __float_as_uint(dot), false, false);
    dot = __uint_as_float(sw[0]) + __uint_as_float(sw[1]);
    if (node < wa1) {
      if (fi == 0) {
        const u32 a = l0 + L_HM + node * 144 + 8 * hh;
#pragma unroll
        for (int q = 0; q < 4; ++q) {
          stl<u32x2>(a + 16 * q, u32x2{cvtpk(Y0[4 * q], Y0[4 * q + 1]), cvtpk(Y0[4 * q + 2], Y0[4 * q + 3])});
          stl<u32x2>(a + 64 + 16 * q, u32x2{cvtpk(Y1[4 * q], Y1[4 * q + 1]), cvtpk(Y1[4 * q + 2], Y1[4 * q + 3])});
        }
      } else if (hh == 0) stl<float>(l0 + L_PA + 16 * node + 4 * (fi - 1), dot);
    }
#pragma unroll
    for (int r = 0; r < 16; ++r) { Y0[r] = 0.f; Y1[r] = 0.f; }
  };

  for (u32 t = 0; t < wnt; ++t) {
    const u32 slot = slot0 + (t & 1) * 4096;
    if (t + 1 < wnt) asm volatile("s_waitcnt vmcnt(4)" ::: "memory"); else asm volatile("s_waitcnt vmcnt(0)" ::: "memory");
    const u32 dsb = ldl<unsigned char>(a_dsb), dsw = ldl<u32>(a_dsw), de = ldl<u32>(a_de);
    bf16x8 As[4], Ad[4];
#pragma unroll
    for (int s = 0; s < 4; ++s) As[s] = ldl<bf16x8>(slot + sfa[s]);
    u32x2 sv = {0, 0}; if (t + 2 < wnt) sv = ldl<u32x2>(a_sr);
    const u32 hrow = hmb + dsb * 144;
#pragma unroll
    for (int s = 0; s < 4; ++s) Ad[s] = ldl<bf16x8>(hrow + 32 * s);
    asm volatile("s_waitcnt lgkmcnt(0)" ::: "memory");
    if (t + 2 < wnt) issue(sv, slot);
    f32x16 X0, X1;
#pragma unroll
    for (int r = 0; r < 16; ++r) { X0[r] = 0.f; X1[r] = 0.f; }
    const bf16x8 Ae = __builtin_bit_cast(bf16x8, u32x4{de, 0, 0, 0});
#pragma unroll
    for (int s = 0; s < 4; ++s) { X0 = MFMA(As[s], W[8 + 2 * s], X0); X1 = MFMA(As[s], W[9 + 2 * s], X1); }
#pragma unroll
    for (int s = 0; s < 4; ++s) { X0 = MFMA(Ad[s], W[2 * s], X0); X1 = MFMA(Ad[s], W[2 * s + 1], X1); }
    X0 = MFMA(Ae, W[16], X0); X1 = MFMA(Ae, W[17], X1);
    u32x4 P0[2], P1[2];
#pragma unroll
    for (int r = 0; r < 16; r += 2) {
      float a0, a1, b0, b1;
      if (FOLD) {
        a0 = X0[r] * __builtin_amdgcn_rcpf(1.f + __builtin_amdgcn_exp2f(X0[r])); a1 = X0[r + 1] * __builtin_amdgcn_rcpf(1.f + __builtin_amdgcn_exp2f(X0[r + 1]));
        b0 = X1[r] * __builtin_amdgcn_rcpf(1.f + __builtin_amdgcn_exp2f(X1[r])); b1 = X1[r + 1] * __builtin_amdgcn_rcpf(1.f + __builtin_amdgcn_exp2f(X1[r + 1]));
      } else { a0 = silu_f(X0[r]); a1 = silu_f(X0[r + 1]); b0 = silu_f(X1[r]); b1 = silu_f(X1[r + 1]); }
      P0[r >> 3][(r >> 1) & 3] = cvtpk(a0, a1); P1[r >> 3][(r >> 1) & 3] = cvtpk(b0, b1);
    }
    const u32 last = (u32)__builtin_amdgcn_readlane(dsw, 44) >> 24;
    for (;;) {
      const u32 tgt = base + slotA;
      s16x4 Bq[4];
#pragma unroll
      for (int a = 0; a < 4; ++a) Bq[a] = ldtr((((dsw >> (8 * a)) & 0xff) == tgt ? a_v : zaddr) + 64 * a);
      const bf16x8 B0 = __builtin_shufflevector(Bq[0], Bq[1], 0, 1, 2, 3, 4, 5, 6, 7), B1 = __builtin_shufflevector(Bq[2], Bq[3], 0, 1, 2, 3, 4, 5, 6, 7);
      Y0 = MFMA(__builtin_bit_cast(bf16x8, P0[0]), B0, Y0); Y1 = MFMA(__builtin_bit_cast(bf16x8, P1[0]), B0, Y1);
      Y0 = MFMA(__builtin_bit_cast(bf16x8, P0[1]), B1, Y0); Y1 = MFMA(__builtin_bit_cast(bf16x8, P1[1]), B1, Y1);
      if (last >= base + 8 && base + 8 < wa1) { flush(); base += 8; } else break;
    }
    a_dsb += 32; a_dsw += 32; a_de += 128; a_v += 256; a_sr += 64;
  }
  {
    const u32 rb = (u32)(n0 + 32 * w) + (lane >> 3), nmax = (u32)N - 1;
    dma4(min(rb, nmax) * 128 + dch0, min(rb + 8, nmax) * 128 + dch1, min(rb + 16, nmax) * 128 + dch0, min(rb + 24, nmax) * 128 + dch1, dh, slot0);
    u32 sv; const char* g = ws + W_WH + w * 2048; const u32 vo = lane * 16;
    asm volatile("s_mov_b32 %0, m0\n\ts_mov_b32 m0, %3\n\ts_nop 0\n\tglobal_load_lds_dwordx4 %1, %2\n\tglobal_load_lds_dwordx4 %1, %2 offset:1024\n\ts_mov_b32 m0, %0"
                 : "=&s"(sv) : "v"(vo), "s"(g), "s"(slot0 + 4096) : "memory");
  }
  while (base < wa1) { flush(); base += 8; }
  asm volatile("s_waitcnt vmcnt(0) lgkmcnt(0)\n\ts_barrier" ::: "memory");

  if (w < 7) {
    const u32 nl = 32 * w + e32, mrow = l0 + L_HM + min(nl, (u32)NPB) * 144 + 16 * hh;
    f32x16 A0, A1, C0, C1;
#pragma unroll
    for (int r = 0; r < 16; ++r) { A0[r] = 0.f; A1[r] = 0.f; C0[r] = 0.f; C1[r] = 0.f; }
#pragma unroll
    for (int s = 0; s < 4; ++s) {
      const bf16x8 hb = ldl<bf16x8>(slot0 + sfa[s]), mb = ldl<bf16x8>(mrow + 32 * s);
      const u32 wa = l0 + L_STG + 4096 + lane * 16;
      const bf16x8 w0 = ldl<bf16x8>(wa + s * 8192), w1 = ldl<bf16x8>(wa + s * 8192 + 1024);
      const bf16x8 w2 = ldl<bf16x8>(wa + (4 + s) * 8192), w3 = ldl<bf16x8>(wa + (4 + s) * 8192 + 1024);
      A0 = MFMA(w0, hb, A0); A1 = MFMA(w1, hb, A1); C0 = MFMA(w2, mb, C0); C1 = MFMA(w3, mb, C1);
    }
    const float km = FOLD ? -LN2 : 1.f;
#pragma unroll
    for (int ft = 0; ft < 2; ++ft)
#pragma unroll
      for (int q = 0; q < 4; ++q) {
        const u32 a = slot0 + e32 * 128 + 16 * ((4 * ft + q) ^ swz) + 8 * hh;
        const u32x2 hv = ldl<u32x2>(a);
        float o[4];
#pragma unroll
        for (int k = 0; k < 4; ++k) { const float z = ft ? A1[4 * q + k] + km * C1[4 * q + k] : A0[4 * q + k] + km * C0[4 * q + k]; o[k] = silu_f(z); }
        stl<u32x2>(a, u32x2{cvtpk(lo2f(hv.x) + o[0], hi2f(hv.x) + o[1]), cvtpk(lo2f(hv.y) + o[2], hi2f(hv.y) + o[3])});
      }
    asm volatile("s_waitcnt lgkmcnt(0)" ::: "memory");
#pragma unroll
    for (int a = 0; a < 4; ++a) {
      const u32 row = 8 * a + (lane >> 3), nr = 32 * w + row;
      const u32x4 v = ldl<u32x4>(slot0 + a * 1024 + lane * 16);
      if (nr < NPB && n0 + nr < (u32)N) *(u32x4*)((char*)out + (size_t)(n0 + nr) * 128 + ((a & 1) ? dch1 : dch0)) = v;
    }
  } else {
    const int nn = min(NPB, N - n0);
    u32* po = (u32*)(out + (size_t)N * 64 + (size_t)n0 * 3);
    for (int i = lane; 2 * i < 3 * nn; i += 64) {
      const int e0 = 2 * i, e1 = e0 + 1, a0 = e0 / 3, a1 = e1 / 3, c0 = e0 - 3 * a0, c1 = e1 - 3 * a1;
      const float v0 = ldl<float>(l0 + L_PT + 16 * a0 + 4 * c0) + ldl<float>(l0 + L_PA + 16 * a0 + 4 * c0);
      const float v1 = ldl<float>(l0 + L_PT + 16 * a1 + 4 * c1) + ldl<float>(l0 + L_PA + 16 * a1 + 4 * c1);
      po[i] = cvtpk(v0, v1);
    }
  }
}

__global__ void gen_zero(float* p, int n) { int i = blockIdx.x * blockDim.x + threadIdx.x; if (i < n) p[i] = 0.f; }
__global__ void gen_edge(const u16* h, const u16* pos, const u16* ea, const int* src, const int* dst, const u16* we, const u16* wx, float* mi, float* pd, int E) {
  const int lane = threadIdx.x & 63, w = threadIdx.x >> 6, nw = blockDim.x >> 6;
  const float wxf = bf2f(wx[lane]);
  for (int e = blockIdx.x * nw + w; e < E; e += gridDim.x * nw) {
    const int s = src[e], d = dst[e];
    float df[3], d2 = 0.f;
#pragma unroll
    for (int k = 0; k < 3; ++k) { df[k] = lo2f(cvtpk(bf2f(pos[(size_t)d * 3 + k]) - bf2f(pos[(size_t)s * 3 + k]), 0.f)); d2 += df[k] * df[k]; }
    d2 = lo2f(cvtpk(d2, 0.f));
    const float hd = bf2f(h[(size_t)d * 64 + lane]), hs = bf2f(h[(size_t)s * 64 + lane]);
    float z = 0.f;
#pragma unroll 4
    for (int k = 0; k < 64; ++k) z += __shfl(hd, k) * bf2f(we[k * 64 + lane]) + __shfl(hs, k) * bf2f(we[(64 + k) * 64 + lane]);
    z += d2 * bf2f(we[128 * 64 + lane]) + bf2f(ea[e]) * bf2f(we[129 * 64 + lane]);
    const float m = lo2f(cvtpk(silu_f(z), 0.f));
    float cs = m * wxf;
#pragma unroll
    for (int o = 32; o > 0; o >>= 1) cs += __shfl_xor(cs, o);
    atomicAdd(&mi[(size_t)d * 64 + lane], m);
    if (lane < 3) atomicAdd(&pd[(size_t)d * 3 + lane], (lane == 0 ? df[0] : lane == 1 ? df[1] : df[2]) * cs);
  }
}
__global__ void gen_node(const u16* h, const u16* pos, const u16* wh, const float* mi, const float* pd, u16* out, int N) {
  const int lane = threadIdx.x & 63, w = threadIdx.x >> 6, nw = blockDim.x >> 6;
  for (int n = blockIdx.x * nw + w; n < N; n += gridDim.x * nw) {
    const float hv = bf2f(h[(size_t)n * 64 + lane]), mv = lo2f(cvtpk(mi[(size_t)n * 64 + lane], 0.f));
    float z = 0.f;
#pragma unroll 4
    for (int k = 0; k < 64; ++k) z += __shfl(hv, k) * bf2f(wh[k * 64 + lane]) + __shfl(mv, k) * bf2f(wh[(64 + k) * 64 + lane]);
    out[(size_t)n * 64 + lane] = (u16)cvtpk(hv + silu_f(z), 0.f);
    if (lane < 3) out[(size_t)N * 64 + (size_t)n * 3 + lane] = (u16)cvtpk(bf2f(pos[(size_t)n * 3 + lane]) + pd[(size_t)n * 3 + lane], 0.f);
  }
}

extern "C" void kernel_launch(void* const* d_in, const int* in_sizes, int n_in, void* d_out, int out_size, void* d_ws, size_t ws_size, hipStream_t stream) {
  const u16* h = (const u16*)d_in[0];
  const u16* pos = (const u16*)d_in[1];
  const u16* ea = (const u16*)d_in[2];
  const int* src = (const int*)d_in[3];
  const int* dst = (const int*)d_in[4];
  const u16* we = (const u16*)d_in[5];
  const u16* wx = (const u16*)d_in[6];
  const u16* wh = (const u16*)d_in[7];
  const int N = in_sizes[0] / 64, E = in_sizes[3];
  const int CS = ((E + NCH - 1) / NCH + 3) & ~3;
  const bool fast = N >= 2 && N <= NPB * NBK && (N & 1) == 0 && (E & 3) == 0 && E > 0 && CS <= 4096 && W_REC + (size_t)NCH * CS * 8 <= ws_size;
  if (fast) {
    (void)hipFuncSetAttribute((const void*)k2_main, hipFuncAttributeMaxDynamicSharedMemorySize, L_END);
    k1_partition<<<NCH + 1, 1024, 0, stream>>>(dst, src, ea, we, wh, (char*)d_ws, E, CS);
    k2_main<<<NBK, 512, L_END, stream>>>(h, pos, we, wx, wh, (const char*)d_ws, (u16*)d_out, N, CS);
  } else {
    float* mi = (float*)d_ws; float* pd = mi + (size_t)N * 64;
    const int nz = N * 67;
    gen_zero<<<(nz + 255) / 256, 256, 0, stream>>>(mi, nz);
    gen_edge<<<2048, 256, 0, stream>>>(h, pos, ea, src, dst, we, wx, mi, pd, E);
    gen_node<<<1024, 256, 0, stream>>>(h, pos, wh, mi, pd, (u16*)d_out, N);
  }
}
```

```cpp
#include <hip/hip_runtime.h>
#include <hip/hip_bf16.h>
#include <stdint.h>

#define PREFETCH 1
#define FOLD 1

typedef unsigned short u16;
typedef unsigned u32;
typedef short bf16x8 __attribute__((ext_vector_type(8)));
typedef short s16x4 __attribute__((ext_vector_type(4)));
typedef float f32x16 __attribute__((ext_vector_type(16)));
typedef float f32x4 __attribute__((ext_vector_type(4)));
typedef unsigned u32x4 __attribute__((ext_vector_type(4)));
typedef unsigned u32x2 __attribute__((ext_vector_type(2)));
#define LAS __attribute__((address_space(3)))

constexpr int NPB = 196;
constexpr int NBK = 256;
constexpr int NCH = 256;
constexpr int MAXT = 112;
constexpr int OVCAP = 512;
constexpr float LOG2E = 1.4426950408889634f, LN2 = 0.6931471805599453f;

constexpr int NT1 = MAXT + 1;
constexpr int L_STG = 0;
constexpr int L_V = 65536;
constexpr int L_DE = L_V + NT1 * 32 * 8;
constexpr int L_SR = L_DE + NT1 * 32 * 4;
constexpr int L_DS = L_SR + NT1 * 32 * 2;
constexpr int L_HM = L_DS + NT1 * 32;
constexpr int L_PT = L_HM + 197 * 144;
constexpr int L_PA = L_PT + NPB * 16;
constexpr int L_CNT = L_PA + NPB * 16;
constexpr int L_OFF = L_CNT + 1024;
constexpr int L_WX = L_OFF + 1024;
constexpr int L_Z = L_WX + 256;
constexpr int L_DUM = L_Z + 256;
constexpr int L_MISC = L_DUM + 256;
constexpr int L_OV = L_MISC + 64;
constexpr int L_END = L_OV + OVCAP * 4;
static_assert(L_HM % 16 == 0 && L_PT % 16 == 0 && L_CNT % 16 == 0 && L_Z % 16 == 0 && L_OV % 16 == 0, "align");
static_assert(L_END <= 160 * 1024, "lds");

constexpr size_t W_TAB = 0;
constexpr size_t W_WE = W_TAB + NCH * NBK * 4;
constexpr size_t W_WH = W_WE + 18 * 1024;
constexpr size_t W_P8 = W_WH + 16 * 1024;
constexpr size_t W_REC = W_P8 + NPB * NBK * 8;

static __device__ __forceinline__ float bf2f(u32 v) { return __uint_as_float(v << 16); }
static __device__ __forceinline__ u32 cvtpk(float lo, float hi) { u32 r; asm("v_cvt_pk_bf16_f32 %0, %1, %2" : "=v"(r) : "v"(lo), "v"(hi)); return r; }
static __device__ __forceinline__ float lo2f(u32 v) { return __uint_as_float(v << 16); }
static __device__ __forceinline__ float hi2f(u32 v) { return __uint_as_float(v & 0xffff0000u); }
template <int CTRL, int RM = 0xf> static __device__ __forceinline__ u32 dpp0(u32 x) { return (u32)__builtin_amdgcn_update_dpp(0, (int)x, CTRL, RM, 0xf, true); }
static __device__ __forceinline__ u32 wave_incl_scan(u32 x) {
  x += dpp0<0x111>(x); x += dpp0<0x112>(x); x += dpp0<0x114>(x); x += dpp0<0x118>(x);
  x += dpp0<0x142, 0xa>(x); x += dpp0<0x143, 0xc>(x); return x; }
#define MFMA(a, b, c) __builtin_amdgcn_mfma_f32_32x32x16_bf16(a, b, c, 0, 0, 0)
#define LBAR() asm volatile("s_waitcnt lgkmcnt(0)\n\ts_barrier" ::: "memory")
template <class T> static __device__ __forceinline__ T ldl(u32 a) { return *(LAS const T*)(size_t)a; }
template <class T> static __device__ __forceinline__ void stl(u32 a, T v) { *(LAS T*)(size_t)a = v; }
static __device__ __forceinline__ s16x4 ldtr(u32 a) { typedef short v4 __attribute__((ext_vector_type(4)));
  return __builtin_bit_cast(s16x4, __builtin_amdgcn_ds_read_tr16_b64_v4i16((LAS v4*)(size_t)a)); }
static __device__ __forceinline__ float silu_f(float z) { return z * __builtin_amdgcn_rcpf(1.f + __builtin_amdgcn_exp2f(-LOG2E * z)); }

#define STAMP(i)
#define STAMP_DECL
#define STAMP_DUMP(base, idx)
__global__ void __launch_bounds__(1024) k1_partition(const int* __restrict__ dst, const int* __restrict__ src, const u16* __restrict__ ea,
                                                    const u16* __restrict__ we, const u16* __restrict__ wh, const u16* __restrict__ pos, const u16* __restrict__ h,
                                                    char* __restrict__ ws, int N, int E, int CS) {
  __shared__ u32x2 srec[4096];
  __shared__ __attribute__((aligned(16))) u32 cnt[NBK];
  __shared__ __attribute__((aligned(16))) u32 start[NBK];
  const int tid = threadIdx.x, c = blockIdx.x;
  STAMP_DECL STAMP(0);
  const int e0 = c * CS + 4 * tid;
  const bool act = 4 * tid < CS && e0 < E;
  u32x4 d4 = {0, 0, 0, 0}, s4 = {0, 0, 0, 0}; u32x2 a4 = {0, 0};
  if (act) { d4 = *(const u32x4*)(dst + e0); s4 = *(const u32x4*)(src + e0); a4 = *(const u32x2*)(ea + e0); }
  const int pn = c * NPB + tid - (1024 - NPB);
  const bool pact = tid >= 1024 - NPB && pn < N;
  u32 pv[3] = {0, 0, 0};
  if (pact) { pv[0] = pos[pn * 3]; pv[1] = pos[pn * 3 + 1]; pv[2] = pos[pn * 3 + 2]; }
  const bool wact = c < 34 && tid >= 960;
  u32 wv[8];
  if (wact) {
    const int l = tid - 960, isH = c >= 18, g = isH ? c - 18 : c;
    const u16* w = (isH ? wh : we) + 32 * (g & 1) + (l & 31); const int k0 = 16 * (g >> 1) + 8 * (l >> 5), kmax = isH ? 127 : 129;
#pragma unroll
    for (int j = 0; j < 8; ++j) wv[j] = w[min(k0 + j, kmax) * 64];
  }
  u32x4 pf0, pf1;
  { const int hb = ((c & 7) * 32 + (c >> 3)) * NPB * 8, hmax = N * 8 - 1;
    pf0 = ((const u32x4*)h)[min(hb + tid, hmax)]; pf1 = ((const u32x4*)h)[min(hb + 1024 + (tid & 511) + (tid >> 9) * 32, hmax)]; }
  if (tid < NBK) cnt[tid] = 0;
  __syncthreads();
  STAMP(1);
  u32 b[4], r[4];
  if (act) {
#pragma unroll
    for (int j = 0; j < 4; ++j) { b[j] = d4[j] / (u32)NPB; r[j] = atomicAdd(&cnt[b[j]], 1u); }
  }
  __syncthreads();
  STAMP(2);
  if (tid < 64) {
    const u32x4 cv = *(const u32x4*)&cnt[4 * tid];
    const u32 s1 = cv.x + cv.y, s2 = s1 + cv.z, s3 = s2 + cv.w;
    const u32 ex = wave_incl_scan(s3) - s3;
    u32x4 st = {ex, ex + cv.x, ex + s1, ex + s2};
    *(u32x4*)&start[4 * tid] = st;
    u32x4 t = {st.x | cv.x << 16, st.y | cv.y << 16, st.z | cv.z << 16, st.w | cv.w << 16};
    *(u32x4*)(ws + W_TAB + ((size_t)c * NBK + 4 * tid) * 4) = t;
  }
  __syncthreads();
  STAMP(3);
  if (act) {
#pragma unroll
    for (int j = 0; j < 4; ++j) {
      const u32 eav = (j & 1) ? (a4[j >> 1] & 0xffff0000u) : (a4[j >> 1] << 16);
      u32x2 rec = {s4[j] | (d4[j] - b[j] * NPB) << 16, eav};
      srec[start[b[j]] + r[j]] = rec;
    }
  }
  __syncthreads();
  STAMP(4);
  const int n = min(CS, E - c * CS);
  u32x4* o = (u32x4*)(ws + W_REC + (size_t)c * CS * 8);
  for (int i = tid; 2 * i < n; i += 1024) o[i] = *(const u32x4*)&srec[2 * i];
  if (pact) *(u32x2*)(ws + W_P8 + (size_t)pn * 8) = u32x2{pv[0] | pv[1] << 16, pv[2]};
  if (wact) {
    const int l = tid - 960, isH = c >= 18, g = isH ? c - 18 : c, k0 = 16 * (g >> 1) + 8 * (l >> 5), kmax = isH ? 127 : 129;
    float v[8];
#pragma unroll
    for (int j = 0; j < 8; ++j) v[j] = k0 + j <= kmax ? bf2f(wv[j]) * ((FOLD && !isH) ? -LOG2E : 1.f) : 0.f;
    *(u32x4*)(ws + W_WE + (size_t)(c * 64 + l) * 16) = u32x4{cvtpk(v[0], v[1]), cvtpk(v[2], v[3]), cvtpk(v[4], v[5]), cvtpk(v[6], v[7])};
  }
  asm volatile("" :: "v"(pf0), "v"(pf1));
  STAMP(5);
  STAMP_DUMP(128u << 20, c * 16 + (tid >> 6));
}

struct Dma { const char* b0; const char* b1; const char* b2; const char* b3; };
static __device__ __forceinline__ void dma4(u32 v0, u32 v1, u32 v2, u32 v3, const Dma& d, u32 lds) {
  u32 sv;
  asm volatile("s_mov_b32 %0, m0\n\ts_mov_b32 m0, %9\n\ts_nop 0\n\t"
               "global_load_lds_dwordx4 %1, %5\n\t"
               "global_load_lds_dwordx4 %2, %6 offset:1024\n\t"
               "global_load_lds_dwordx4 %3, %7 offset:2048\n\t"
               "global_load_lds_dwordx4 %4, %8 offset:3072\n\t"
               "s_mov_b32 m0, %0"
               : "=&s"(sv) : "v"(v0), "v"(v1), "v"(v2), "v"(v3), "s"(d.b0), "s"(d.b1), "s"(d.b2), "s"(d.b3), "s"(lds) : "memory");
}

static __device__ __forceinline__ void slow_bucket(const u16* __restrict__ h, const u16* __restrict__ pos, const u16* __restrict__ we, const u16* __restrict__ wx,
                                                const u16* __restrict__ wh, const char* __restrict__ ws, u16* __restrict__ out, int N, int CS, int bk, u32 l0) {
  const int tid = threadIdx.x, lane = tid & 63, w = tid >> 6, n0 = bk * NPB;
  LAS float* macc = (LAS float*)(size_t)(l0 + L_STG);
  LAS float* pacc = (LAS float*)(size_t)(l0 + L_PA);
  __syncthreads();
  for (int i = tid; i < NPB * 64; i += 512) macc[i] = 0.f;
  for (int i = tid; i < NPB * 4; i += 512) pacc[i] = 0.f;
  __syncthreads();
  const float wxf = bf2f(wx[lane]);
  for (int c = w; c < NCH; c += 8) {
    const u32 ent = *(const u32*)(ws + W_TAB + ((size_t)c * NBK + bk) * 4);
    const int st = ent & 0xffff, cn = ent >> 16;
    for (int j = 0; j < cn; ++j) {
      const u32x2 rec = *(const u32x2*)(ws + W_REC + ((size_t)c * CS + st + j) * 8);
      const int s = rec.x & 0xffff, dl = rec.x >> 16, d = n0 + dl;
      float df[3], d2 = 0.f;
#pragma unroll
      for (int k = 0; k < 3; ++k) { df[k] = lo2f(cvtpk(bf2f(pos[d * 3 + k]) - bf2f(pos[s * 3 + k]), 0.f)); d2 += df[k] * df[k]; }
      d2 = lo2f(cvtpk(d2, 0.f));
      const float hd = bf2f(h[(size_t)d * 64 + lane]), hs = bf2f(h[(size_t)s * 64 + lane]);
      float z = 0.f;
#pragma unroll 4
      for (int k = 0; k < 64; ++k) z += __shfl(hd, k) * bf2f(we[k * 64 + lane]) + __shfl(hs, k) * bf2f(we[(64 + k) * 64 + lane]);
      z += d2 * bf2f(we[128 * 64 + lane]) + hi2f(rec.y) * bf2f(we[129 * 64 + lane]);
      const float m = lo2f(cvtpk(silu_f(z), 0.f));
      float cs = m * wxf;
#pragma unroll
      for (int o = 32; o > 0; o >>= 1) cs += __shfl_xor(cs, o);
      atomicAdd((float*)&macc[dl * 64 + lane], m);
      if (lane < 3) atomicAdd((float*)&pacc[dl * 4 + lane], (lane == 0 ? df[0] : lane == 1 ? df[1] : df[2]) * cs);
    }
  }
  __syncthreads();
  for (int nl = w; nl < NPB; nl += 8) {
    const int n = n0 + nl; if (n >= N) break;
    const float hv = bf2f(h[(size_t)n * 64 + lane]), mv = lo2f(cvtpk(macc[nl * 64 + lane], 0.f));
    float z = 0.f;
#pragma unroll 4
    for (int k = 0; k < 64; ++k) z += __shfl(hv, k) * bf2f(wh[k * 64 + lane]) + __shfl(mv, k) * bf2f(wh[(64 + k) * 64 + lane]);
    out[(size_t)n * 64 + lane] = (u16)cvtpk(hv + silu_f(z), 0.f);
    if (lane < 3) out[(size_t)N * 64 + (size_t)n * 3 + lane] = (u16)cvtpk(bf2f(pos[n * 3 + lane]) + pacc[nl * 4 + lane], 0.f);
  }
}

__global__ void __launch_bounds__(512, 2) k2_main(const u16* __restrict__ h, const u16* __restrict__ pos, const u16* __restrict__ we, const u16* __restrict__ wx,
                                                  const u16* __restrict__ wh, const char* __restrict__ ws, u16* __restrict__ out, int N, int CS) {
  extern __shared__ __attribute__((aligned(16))) char smem[];
  const u32 l0 = (u32)(size_t)(LAS char*)smem;
  const int tid = threadIdx.x, lane = tid & 63;
  const int w = __builtin_amdgcn_readfirstlane(tid >> 6);
  const int bk = (blockIdx.x & 7) * 32 + (blockIdx.x >> 3);
  const int n0 = bk * NPB;
  if (n0 >= N) return;
  STAMP_DECL STAMP(0);

  const int grp = lane >> 4, j16 = lane & 15;
  const int chunk0 = 32 * w + 8 * grp;
  const u32 ent = *(const u32*)(ws + W_TAB + ((size_t)(chunk0 + (j16 & 7)) * NBK + bk) * 4);
  const u32x2* p8 = (const u32x2*)(ws + W_P8);
  const int nmax = N - 1;
  u32x4 ht[4], wt[3];
#pragma unroll
  for (int i = 0; i < 4; ++i) { const int pc = tid + 512 * i; ht[i] = *(const u32x4*)(h + (size_t)min(n0 + (pc >> 3), nmax) * 64 + (pc & 7) * 8); }
#pragma unroll
  for (int i = 0; i < 3; ++i) wt[i] = *(const u32x4*)(ws + W_WE + (size_t)min(tid + 512 * i, 18 * 64 - 1) * 16);
  const u32x2 ptile = p8[min(n0 + tid, nmax)];
  const u32 wxr = wx[lane];
  if (tid < 256) stl<u32>(l0 + L_CNT + 4 * tid, 0u);
  if (tid < 16) stl<u32>(l0 + L_MISC + 4 * tid, tid < 8 ? 0xffffffffu : 0u);
  if (tid < 64) stl<u32>(l0 + L_Z + 4 * tid, 0u);
  if (tid < NPB) stl<f32x4>(l0 + L_PA + 16 * tid, f32x4{0, 0, 0, 0});
  LBAR();
  STAMP(1);

  u32x2 rec[9]; u32 vmask = 0;
  u32 segb[8], segc[8];
  const u32 sb0 = (u32)chunk0 * (u32)CS + j16;
#define SEG_ENT(i) { const u32 e_ = dpp0<0x150 + i>(ent); segc[i] = e_ >> 16; segb[i] = sb0 + (u32)(i * CS) + (e_ & 0xffff); }
  SEG_ENT(0) SEG_ENT(1) SEG_ENT(2) SEG_ENT(3) SEG_ENT(4) SEG_ENT(5) SEG_ENT(6) SEG_ENT(7)
  const u32x2* recs = (const u32x2*)(ws + W_REC);
#pragma unroll
  for (int i = 0; i < 8; ++i) { const bool v = (u32)j16 < segc[i]; rec[i] = recs[v ? segb[i] : 0u]; vmask |= (v ? 1u : 0u) << i; }
  {
    u32 ex[8], tot = 0, mx = 0;
#pragma unroll
    for (int i = 0; i < 8; ++i) { ex[i] = segc[i] > 16 ? segc[i] - 16 : 0; tot += ex[i]; mx = max(mx, ex[i]); }
    u32 gb = 0;
    if (j16 == 0) gb = __hip_atomic_fetch_add((LAS u32*)(size_t)(l0 + L_MISC + 36), tot, __ATOMIC_RELAXED, __HIP_MEMORY_SCOPE_WORKGROUP);
    gb = dpp0<0x150>(gb) + j16;
    if (__builtin_amdgcn_ballot_w64(mx > 16) == 0) {
#pragma unroll
      for (int i = 0; i < 8; ++i) { stl<u32>(((u32)j16 < ex[i] && gb < OVCAP) ? l0 + L_OV + 4 * gb : l0 + L_DUM + 4 * lane, segb[i] + 16); gb += ex[i]; }
    } else {
#pragma unroll
      for (int i = 0; i < 8; ++i) { for (u32 k = 0; k + j16 < ex[i]; k += 16) if (gb + k < OVCAP) stl<u32>(l0 + L_OV + 4 * (gb + k), segb[i] + 16 + k); gb += ex[i]; }
    }
  }
#pragma unroll
  for (int i = 0; i < 4; ++i) { const int pc = tid + 512 * i, row = pc >> 3;
    if (i < 3 || row <= NPB) stl<u32x4>(l0 + L_HM + row * 144 + (pc & 7) * 16, (i == 3 && row == NPB) ? u32x4{0, 0, 0, 0} : ht[i]); }
#pragma unroll
  for (int i = 0; i < 3; ++i) { const int pc = tid + 512 * i; if (i < 2 || pc < 18 * 64) stl<u32x4>(l0 + L_STG + pc * 16, wt[i]); }
  if (tid < NPB) stl<f32x4>(l0 + L_PT + 16 * tid, f32x4{lo2f(ptile.x), hi2f(ptile.x), lo2f(ptile.y), 0.f});
  if (tid < 64) stl<float>(l0 + L_WX + 4 * tid, bf2f(wxr) * (FOLD ? -LN2 : 1.f));
  LBAR();
  const u32 novf = __builtin_amdgcn_readfirstlane(ldl<u32>(l0 + L_MISC + 36));
  { const bool v = (u32)tid < novf; const u32 idx = ldl<u32>(l0 + L_OV + 4 * tid); rec[8] = recs[v ? idx : 0u]; vmask |= (v ? 1u : 0u) << 8; }
  STAMP(2);
  u32 rank[9]; u32x2 ps[9];
#pragma unroll
  for (int i = 0; i < 9; ++i) {
    ps[i] = p8[rec[i].x & 0xffff];
    rank[i] = __hip_atomic_fetch_add((LAS u32*)(size_t)((vmask >> i & 1) ? l0 + L_CNT + 4 * (rec[i].x >> 16) : l0 + L_DUM + 4 * lane), 1u, __ATOMIC_RELAXED, __HIP_MEMORY_SCOPE_WORKGROUP);
  }
  STAMP(3);
  LBAR();
  STAMP(4);
  bf16x8 W[18];
#pragma unroll
  for (int i = 0; i < 18; ++i) W[i] = ldl<bf16x8>(l0 + L_STG + i * 1024 + lane * 16);
  u32 wa0, wa1, wtb, wnt, wend; bool irregular;
  {
    const u32x4 cv = ldl<u32x4>(l0 + L_CNT + 16 * lane);
    const u32 s1 = cv.x + cv.y, s2 = s1 + cv.z, s3 = s2 + cv.w;
    const u32 inc = wave_incl_scan(s3), ex = inc - s3;
    const u32 T = __builtin_amdgcn_readlane(inc, 63);
    const u32 off[4] = {ex, ex + cv.x, ex + s1, ex + s2};
    const float rT = T ? 8.f / (float)T : 0.f;
    u32 wv[4];
#pragma unroll
    for (int i = 0; i < 4; ++i) wv[i] = min(7u, (u32)((float)off[i] * rT));
    u32 prev = (u32)__builtin_amdgcn_update_dpp(-1, (int)wv[3], 0x138, 0xf, 0xf, false);
#pragma unroll
    for (int i = 0; i < 4; ++i) { const u32 n = 4 * lane + i;
      if (n < NPB && wv[i] != prev) stl<u32>(l0 + L_MISC + 4 * wv[i], off[i] << 8 | n);
      prev = wv[i]; }
    u32 cut = min(ldl<u32>(l0 + L_MISC + 4 * (lane & 7)), T << 8 | NPB);
    if (j16 >= 8) cut = T << 8 | NPB;
    cut = min(cut, (u32)__builtin_amdgcn_update_dpp(-1, (int)cut, 0x101, 0xf, 0xf, false));
    cut = min(cut, (u32)__builtin_amdgcn_update_dpp(-1, (int)cut, 0x102, 0xf, 0xf, false));
    cut = min(cut, (u32)__builtin_amdgcn_update_dpp(-1, (int)cut, 0x104, 0xf, 0xf, false));
    const u32 nxt = (u32)__builtin_amdgcn_update_dpp(-1, (int)cut, 0x101, 0xf, 0xf, false);
    u32 len = j16 < 8 ? (nxt >> 8) - (cut >> 8) : 0, nt = (len + 31) >> 5;
    u32 tb = nt; tb += dpp0<0x111>(tb); tb += dpp0<0x112>(tb); tb += dpp0<0x114>(tb);
    const u32 ttot = __builtin_amdgcn_readlane(tb, 7);
    tb -= nt;
    const u32 delta = 32 * tb - (cut >> 8);
    u32x4 op;
#pragma unroll
    for (int i = 0; i < 4; ++i) op[i] = off[i] + (u32)__builtin_amdgcn_ds_bpermute(4 * wv[i], (int)delta);
    stl<u32x4>(l0 + L_OFF + 16 * lane, op);
    wa0 = __builtin_amdgcn_readlane(cut, w) & 0xff; wa1 = __builtin_amdgcn_readlane(nxt, w) & 0xff;
    wtb = __builtin_amdgcn_readlane(tb, w); wnt = __builtin_amdgcn_readlane(nt, w);
    wend = 32 * wtb + __builtin_amdgcn_readlane(len, w);
    irregular = ttot > MAXT || novf > OVCAP;
  }
  if (irregular) { slow_bucket(h, pos, we, wx, wh, ws, out, N, CS, bk, l0); return; }

  STAMP(5);
  u32 pp[9];
#pragma unroll
  for (int i = 0; i < 9; ++i) pp[i] = ldl<u32>(l0 + L_OFF + 4 * (rec[i].x >> 16));
#pragma unroll
  for (int i = 0; i < 9; ++i) {
    const u32 p = (vmask >> i & 1) ? pp[i] + rank[i] : (u32)(MAXT * 32 + (lane & 31)); pp[i] = p;
    const u32 q = (p & ~31u) | (p & 7) << 2 | (p >> 3 & 3);
    stl<u16>(l0 + L_SR + 2 * q, (u16)rec[i].x); stl<unsigned char>(l0 + L_DS + q, (unsigned char)(rec[i].x >> 16));
  }
  {
    const u32 p = wend + lane;
    if (p < 32 * (wtb + wnt)) {
      const u32 q = (p & ~31u) | (p & 7) << 2 | (p >> 3 & 3);
      stl<u16>(l0 + L_SR + 2 * q, (u16)0); stl<unsigned char>(l0 + L_DS + q, (unsigned char)NPB);
      stl<u32x2>(l0 + L_V + 8 * p, u32x2{0, 0}); stl<u32>(l0 + L_DE + 4 * p, 0u);
    }
  }
  LBAR();
  STAMP(6);
  const Dma dh = {(const char*)h, (const char*)h - 1024, (const char*)h - 2048, (const char*)h - 3072};
  const u32 slot0 = l0 + L_STG + w * 8192;
  const u32 dch0 = 16 * ((lane & 7) ^ (lane >> 4)), dch1 = dch0 ^ 64;
  const u32 sraddr = l0 + L_SR + 64 * wtb + 8 * (lane >> 3);
  auto issue = [&](u32x2 sv, u32 slot) {
    dma4((sv.x & 0xffff) * 128 + dch0, (sv.x >> 16) * 128 + dch1, (sv.y & 0xffff) * 128 + dch0, (sv.y >> 16) * 128 + dch1, dh, slot);
  };
#pragma unroll
  for (int i = 0; i < 9; ++i) asm volatile("" : "+v"(ps[i].x), "+v"(ps[i].y));
  if (wnt > 0) issue(ldl<u32x2>(sraddr), slot0);
  if (wnt > 1) issue(ldl<u32x2>(sraddr + 64), slot0 + 4096);
  {
    f32x4 pd[9];
#pragma unroll
    for (int i = 0; i < 9; ++i) pd[i] = ldl<f32x4>(l0 + L_PT + 16 * (rec[i].x >> 16));
#pragma unroll
    for (int i = 0; i < 9; ++i) {
      const float dx = pd[i].x - lo2f(ps[i].x), dy = pd[i].y - hi2f(ps[i].x), dz = pd[i].z - lo2f(ps[i].y);
      const float d2 = dx * dx + dy * dy + dz * dz;
      stl<u32x2>(l0 + L_V + 8 * pp[i], u32x2{cvtpk(1.f, dx), cvtpk(dy, dz)});
      stl<u32>(l0 + L_DE + 4 * pp[i], (cvtpk(d2, 0.f) & 0xffffu) | rec[i].y);
    }
  }
  STAMP(7);
  LBAR();
  STAMP(8);

  const int e32 = lane & 31, hh = lane >> 5;
  const u32 swz = (e32 >> 1) & 7;
  u32 sfa[4];
#pragma unroll
  for (int s = 0; s < 4; ++s) sfa[s] = e32 * 128 + 16 * ((2 * s + hh) ^ swz);
  const u32 eq = 4 * hh + ((lane >> 2) & 3), slotA = 4 * ((lane >> 4) & 1) + (lane & 3);
  const u32 zaddr = l0 + L_Z;
  u32 a_dsb = l0 + L_DS + 32 * wtb + 4 * (e32 & 7) + (e32 >> 3);
  u32 a_dsw = l0 + L_DS + 32 * wtb + 4 * eq;
  u32 a_de = l0 + L_DE + 128 * wtb + 4 * e32;
  u32 a_v = l0 + L_V + 256 * wtb + 8 * eq;
  u32 a_sr = sraddr + 128;
  const u32 hmb = l0 + L_HM + 16 * hh;
  f32x16 Y0, Y1;
#pragma unroll
  for (int r = 0; r < 16; ++r) { Y0[r] = 0.f; Y1[r] = 0.f; }
  u32 base = wa0;
  const u32 fn = (lane & 31) >> 2, fi = lane & 3;
  auto flush = [&]() {
    const u32 node = base + fn;
    float dot = 0.f;
#pragma unroll
    for (int q = 0; q < 4; ++q) {
      const f32x4 w0 = ldl<f32x4>(l0 + L_WX + 32 * q + 16 * hh), w1 = ldl<f32x4>(l0 + L_WX + 128 + 32 * q + 16 * hh);
#pragma unroll
      for (int k = 0; k < 4; ++k) { dot += Y0[4 * q + k] * w0[k]; dot += Y1[4 * q + k] * w1[k]; }
    }
    auto sw = __builtin_amdgcn_permlane32_swap(__float_as_uint(dot), __float_as_uint(dot), false, false);
    dot = __uint_as_float(sw[0]) + __uint_as_float(sw[1]);
    if (node < wa1) {
      if (fi == 0) {
        const u32 a = l0 + L_HM + node * 144 + 8 * hh;
#pragma unroll
        for (int q = 0; q < 4; ++q) {
          stl<u32x2>(a + 16 * q, u32x2{cvtpk(Y0[4 * q], Y0[4 * q + 1]), cvtpk(Y0[4 * q + 2], Y0[4 * q + 3])});
          stl<u32x2>(a + 64 + 16 * q, u32x2{cvtpk(Y1[4 * q], Y1[4 * q + 1]), cvtpk(Y1[4 * q + 2], Y1[4 * q + 3])});
        }
      } else if (hh == 0) stl<float>(l0 + L_PA + 16 * node + 4 * (fi - 1), dot);
    }
#pragma unroll
    for (int r = 0; r < 16; ++r) { Y0[r] = 0.f; Y1[r] = 0.f; }
  };

  for (u32 t = 0; t < wnt; ++t) {
    const u32 slot = slot0 + (t & 1) * 4096;
    if (t + 1 < wnt) asm volatile("s_waitcnt vmcnt(4)" ::: "memory"); else asm volatile("s_waitcnt vmcnt(0)" ::: "memory");
    const u32 dsb = ldl<unsigned char>(a_dsb), dsw = ldl<u32>(a_dsw), de = ldl<u32>(a_de);
    bf16x8 As[4], Ad[4];
#pragma unroll
    for (int s = 0; s < 4; ++s) As[s] = ldl<bf16x8>(slot + sfa[s]);
    u32x2 sv = {0, 0}; if (t + 2 < wnt) sv = ldl<u32x2>(a_sr);
    const u32 hrow = hmb + dsb * 144;
#pragma unroll
    for (int s = 0; s < 4; ++s) Ad[s] = ldl<bf16x8>(hrow + 32 * s);
    asm volatile("s_waitcnt lgkmcnt(0)" ::: "memory");
    if (t + 2 < wnt) issue(sv, slot);
    f32x16 X0, X1;
#pragma unroll
    for (int r = 0; r < 16; ++r) { X0[r] = 0.f; X1[r] = 0.f; }
    const bf16x8 Ae = __builtin_bit_cast(bf16x8, u32x4{de, 0, 0, 0});
#pragma unroll
    for (int s = 0; s < 4; ++s) { X0 = MFMA(As[s], W[8 + 2 * s], X0); X1 = MFMA(As[s], W[9 + 2 * s], X1); }
#pragma unroll
    for (int s = 0; s < 4; ++s) { X0 = MFMA(Ad[s], W[2 * s], X0); X1 = MFMA(Ad[s], W[2 * s + 1], X1); }
    X0 = MFMA(Ae, W[16], X0); X1 = MFMA(Ae, W[17], X1);
    u32x4 P0[2], P1[2];
#pragma unroll
    for (int r = 0; r < 16; r += 2) {
      float a0, a1, b0, b1;
      if (FOLD) {
        a0 = X0[r] * __builtin_amdgcn_rcpf(1.f + __builtin_amdgcn_exp2f(X0[r])); a1 = X0[r + 1] * __builtin_amdgcn_rcpf(1.f + __builtin_amdgcn_exp2f(X0[r + 1]));
        b0 = X1[r] * __builtin_amdgcn_rcpf(1.f + __builtin_amdgcn_exp2f(X1[r])); b1 = X1[r + 1] * __builtin_amdgcn_rcpf(1.f + __builtin_amdgcn_exp2f(X1[r + 1]));
      } else { a0 = silu_f(X0[r]); a1 = silu_f(X0[r + 1]); b0 = silu_f(X1[r]); b1 = silu_f(X1[r + 1]); }
      P0[r >> 3][(r >> 1) & 3] = cvtpk(a0, a1); P1[r >> 3][(r >> 1) & 3] = cvtpk(b0, b1);
    }
    const u32 last = (u32)__builtin_amdgcn_readlane(dsw, 44) >> 24;
    for (;;) {
      const u32 tgt = base + slotA;
      s16x4 Bq[4];
#pragma unroll
      for (int a = 0; a < 4; ++a) Bq[a] = ldtr((((dsw >> (8 * a)) & 0xff) == tgt ? a_v : zaddr) + 64 * a);
      const bf16x8 B0 = __builtin_shufflevector(Bq[0], Bq[1], 0, 1, 2, 3, 4, 5, 6, 7), B1 = __builtin_shufflevector(Bq[2], Bq[3], 0, 1, 2, 3, 4, 5, 6, 7);
      Y0 = MFMA(__builtin_bit_cast(bf16x8, P0[0]), B0, Y0); Y1 = MFMA(__builtin_bit_cast(bf16x8, P1[0]), B0, Y1);
      Y0 = MFMA(__builtin_bit_cast(bf16x8, P0[1]), B1, Y0); Y1 = MFMA(__builtin_bit_cast(bf16x8, P1[1]), B1, Y1);
      if (last >= base + 8 && base + 8 < wa1) { flush(); base += 8; } else break;
    }
    a_dsb += 32; a_dsw += 32; a_de += 128; a_v += 256; a_sr += 64;
  }
  STAMP(9);
  {
    const u32 rb = (u32)(n0 + 32 * w) + (lane >> 3), nmax = (u32)N - 1;
    dma4(min(rb, nmax) * 128 + dch0, min(rb + 8, nmax) * 128 + dch1, min(rb + 16, nmax) * 128 + dch0, min(rb + 24, nmax) * 128 + dch1, dh, slot0);
    u32 sv; const char* g = ws + W_WH + w * 2048; const u32 vo = lane * 16;
    asm volatile("s_mov_b32 %0, m0\n\ts_mov_b32 m0, %3\n\ts_nop 0\n\tglobal_load_lds_dwordx4 %1, %2\n\tglobal_load_lds_dwordx4 %1, %2 offset:1024\n\ts_mov_b32 m0, %0"
                 : "=&s"(sv) : "v"(vo), "s"(g), "s"(slot0 + 4096) : "memory");
  }
  while (base < wa1) { flush(); base += 8; }
  STAMP(10);
  asm volatile("s_waitcnt vmcnt(0) lgkmcnt(0)\n\ts_barrier" ::: "memory");
  STAMP(11);

  if (w < 7) {
    const u32 nl = 32 * w + e32, mrow = l0 + L_HM + min(nl, (u32)NPB) * 144 + 16 * hh;
    f32x16 A0, A1, C0, C1;
#pragma unroll
    for (int r = 0; r < 16; ++r) { A0[r] = 0.f; A1[r] = 0.f; C0[r] = 0.f; C1[r] = 0.f; }
#pragma unroll
    for (int s = 0; s < 4; ++s) {
      const bf16x8 hb = ldl<bf16x8>(slot0 + sfa[s]), mb = ldl<bf16x8>(mrow + 32 * s);
      const u32 wa = l0 + L_STG + 4096 + lane * 16;
      const bf16x8 w0 = ldl<bf16x8>(wa + s * 8192), w1 = ldl<bf16x8>(wa + s * 8192 + 1024);
      const bf16x8 w2 = ldl<bf16x8>(wa + (4 + s) * 8192), w3 = ldl<bf16x8>(wa + (4 + s) * 8192 + 1024);
      A0 = MFMA(w0, hb, A0); A1 = MFMA(w1, hb, A1); C0 = MFMA(w2, mb, C0); C1 = MFMA(w3, mb, C1);
    }
    const float km = FOLD ? -LN2 : 1.f;
#pragma unroll
    for (int ft = 0; ft < 2; ++ft)
#pragma unroll
      for (int q = 0; q < 4; ++q) {
        const u32 a = slot0 + e32 * 128 + 16 * ((4 * ft + q) ^ swz) + 8 * hh;
        const u32x2 hv = ldl<u32x2>(a);
        float o[4];
#pragma unroll
        for (int k = 0; k < 4; ++k) { const float z = ft ? A1[4 * q + k] + km * C1[4 * q + k] : A0[4 * q + k] + km * C0[4 * q + k]; o[k] = silu_f(z); }
        stl<u32x2>(a, u32x2{cvtpk(lo2f(hv.x) + o[0], hi2f(hv.x) + o[1]), cvtpk(lo2f(hv.y) + o[2], hi2f(hv.y) + o[3])});
      }
    asm volatile("s_waitcnt lgkmcnt(0)" ::: "memory");
#pragma unroll
    for (int a = 0; a < 4; ++a) {
      const u32 row = 8 * a + (lane >> 3), nr = 32 * w + row;
      const u32x4 v = ldl<u32x4>(slot0 + a * 1024 + lane * 16);
      if (nr < NPB && n0 + nr < (u32)N) *(u32x4*)((char*)out + (size_t)(n0 + nr) * 128 + ((a & 1) ? dch1 : dch0)) = v;
    }
  } else {
    const int nn = min(NPB, N - n0);
    u32* po = (u32*)(out + (size_t)N * 64 + (size_t)n0 * 3);
    for (int i = lane; 2 * i < 3 * nn; i += 64) {
      const int e0 = 2 * i, e1 = e0 + 1, a0 = e0 / 3, a1 = e1 / 3, c0 = e0 - 3 * a0, c1 = e1 - 3 * a1;
      const float v0 = ldl<float>(l0 + L_PT + 16 * a0 + 4 * c0) + ldl<float>(l0 + L_PA + 16 * a0 + 4 * c0);
      const float v1 = ldl<float>(l0 + L_PT + 16 * a1 + 4 * c1) + ldl<float>(l0 + L_PA + 16 * a1 + 4 * c1);
      po[i] = cvtpk(v0, v1);
    }
  }
  STAMP(12);
  STAMP_DUMP(160u << 20, bk * 8 + w);
}

__global__ void gen_zero(float* p, int n) { int i = blockIdx.x * blockDim.x + threadIdx.x; if (i < n) p[i] = 0.f; }
__global__ void gen_edge(const u16* h, const u16* pos, const u16* ea, const int* src, const int* dst, const u16* we, const u16* wx, float* mi, float* pd, int E) {
  const int lane = threadIdx.x & 63, w = threadIdx.x >> 6, nw = blockDim.x >> 6;
  const float wxf = bf2f(wx[lane]);
  for (int e = blockIdx.x * nw + w; e < E; e += gridDim.x * nw) {
    const int s = src[e], d = dst[e];
    float df[3], d2 = 0.f;
#pragma unroll
    for (int k = 0; k < 3; ++k) { df[k] = lo2f(cvtpk(bf2f(pos[(size_t)d * 3 + k]) - bf2f(pos[(size_t)s * 3 + k]), 0.f)); d2 += df[k] * df[k]; }
    d2 = lo2f(cvtpk(d2, 0.f));
    const float hd = bf2f(h[(size_t)d * 64 + lane]), hs = bf2f(h[(size_t)s * 64 + lane]);
    float z = 0.f;
#pragma unroll 4
    for (int k = 0; k < 64; ++k) z += __shfl(hd, k) * bf2f(we[k * 64 + lane]) + __shfl(hs, k) * bf2f(we[(64 + k) * 64 + lane]);
    z += d2 * bf2f(we[128 * 64 + lane]) + bf2f(ea[e]) * bf2f(we[129 * 64 + lane]);
    const float m = lo2f(cvtpk(silu_f(z), 0.f));
    float cs = m * wxf;
#pragma unroll
    for (int o = 32; o > 0; o >>= 1) cs += __shfl_xor(cs, o);
    atomicAdd(&mi[(size_t)d * 64 + lane], m);
    if (lane < 3) atomicAdd(&pd[(size_t)d * 3 + lane], (lane == 0 ? df[0] : lane == 1 ? df[1] : df[2]) * cs);
  }
}
__global__ void gen_node(const u16* h, const u16* pos, const u16* wh, const float* mi, const float* pd, u16* out, int N) {
  const int lane = threadIdx.x & 63, w = threadIdx.x >> 6, nw = blockDim.x >> 6;
  for (int n = blockIdx.x * nw + w; n < N; n += gridDim.x * nw) {
    const float hv = bf2f(h[(size_t)n * 64 + lane]), mv = lo2f(cvtpk(mi[(size_t)n * 64 + lane], 0.f));
    float z = 0.f;
#pragma unroll 4
    for (int k = 0; k < 64; ++k) z += __shfl(hv, k) * bf2f(wh[k * 64 + lane]) + __shfl(mv, k) * bf2f(wh[(64 + k) * 64 + lane]);
    out[(size_t)n * 64 + lane] = (u16)cvtpk(hv + silu_f(z), 0.f);
    if (lane < 3) out[(size_t)N * 64 + (size_t)n * 3 + lane] = (u16)cvtpk(bf2f(pos[(size_t)n * 3 + lane]) + pd[(size_t)n * 3 + lane], 0.f);
  }
}

extern "C" void kernel_launch(void* const* d_in, const int* in_sizes, int n_in, void* d_out, int out_size, void* d_ws, size_t ws_size, hipStream_t stream) {
  const u16* h = (const u16*)d_in[0];
  const u16* pos = (const u16*)d_in[1];
  const u16* ea = (const u16*)d_in[2];
  const int* src = (const int*)d_in[3];
  const int* dst = (const int*)d_in[4];
  const u16* we = (const u16*)d_in[5];
  const u16* wx = (const u16*)d_in[6];
  const u16* wh = (const u16*)d_in[7];
  const int N = in_sizes[0] / 64, E = in_sizes[3];
  const int CS = ((E + NCH - 1) / NCH + 3) & ~3;
  const bool fast = N >= 2 && N <= NPB * NBK && (N & 1) == 0 && (E & 3) == 0 && E > 0 && CS <= 4096 && W_REC + (size_t)NCH * CS * 8 <= ws_size;
  if (fast) {
    (void)hipFuncSetAttribute((const void*)k2_main, hipFuncAttributeMaxDynamicSharedMemorySize, L_END);
    k1_partition<<<NCH, 1024, 0, stream>>>(dst, src, ea, we, wh, pos, h, (char*)d_ws, N, E, CS);
    k2_main<<<NBK, 512, L_END, stream>>>(h, pos, we, wx, wh, (const char*)d_ws, (u16*)d_out, N, CS);
  } else {
    float* mi = (float*)d_ws; float* pd = mi + (size_t)N * 64;
    const int nz = N * 67;
    gen_zero<<<(nz + 255) / 256, 256, 0, stream>>>(mi, nz);
    gen_edge<<<2048, 256, 0, stream>>>(h, pos, ea, src, dst, we, wx, mi, pd, E);
    gen_node<<<1024, 256, 0, stream>>>(h, pos, wh, mi, pd, (u16*)d_out, N);
  }
}
```

```cpp
#include <hip/hip_runtime.h>
#include <hip/hip_bf16.h>
#include <stdint.h>

#define PREFETCH 0
#define FOLD 1

typedef unsigned short u16;
typedef unsigned u32;
typedef short bf16x8 __attribute__((ext_vector_type(8)));
typedef short s16x4 __attribute__((ext_vector_type(4)));
typedef float f32x16 __attribute__((ext_vector_type(16)));
typedef float f32x4 __attribute__((ext_vector_type(4)));
typedef unsigned u32x4 __attribute__((ext_vector_type(4)));
typedef unsigned u32x2 __attribute__((ext_vector_type(2)));
#define LAS __attribute__((address_space(3)))

constexpr int NPB = 196;
constexpr int NBK = 256;
constexpr int NCH = 256;
constexpr int MAXT = 112;
constexpr float LOG2E = 1.4426950408889634f, LN2 = 0.6931471805599453f;

constexpr int NT1 = MAXT + 1;
constexpr int RS = 144;
constexpr int XW = 14;
constexpr int L_STG = 0;
constexpr int L_V = 65536;
constexpr int L_DE = L_V + NT1 * 32 * 8;
constexpr int L_SR = L_DE + NT1 * 32 * 4;
constexpr int L_DS = L_SR + NT1 * 32 * 2;
constexpr int L_HM = L_DS + NT1 * 32;
constexpr int L_PT = L_HM + 197 * RS;
constexpr int L_PA = L_PT + NPB * 16;
constexpr int L_CNT = L_PA + NPB * 16;
constexpr int L_OFF = L_CNT + 1024;
constexpr int L_WX = L_OFF + 1024;
constexpr int L_WR = L_WX + 128;
constexpr int L_OH = L_WR + 256;
constexpr int L_Z = L_OH + 256;
constexpr int L_DUM = L_Z + 512;
constexpr int L_MISC = L_DUM + 256;
constexpr int L_OV = L_MISC + 64;
constexpr int L_END = L_OV + 8 * 256;
static_assert(L_HM % 16 == 0 && L_PT % 16 == 0 && L_CNT % 16 == 0 && L_Z % 16 == 0 && L_OV % 16 == 0 && L_WX % 16 == 0 && L_OH % 16 == 0, "align");
static_assert(L_END <= 160 * 1024, "lds");

constexpr size_t W_TAB = 0;
constexpr size_t W_WE = W_TAB + NCH * NBK * 4;
constexpr size_t W_WH = W_WE + 16 * 1024;
constexpr size_t W_P8 = W_WH + 16 * 1024;
constexpr size_t W_REC = W_P8 + NPB * NBK * 8;

static __device__ __forceinline__ float bf2f(u32 v) { return __uint_as_float(v << 16); }
typedef __bf16 bf16x2_t __attribute__((ext_vector_type(2)));
typedef float f32x2 __attribute__((ext_vector_type(2)));
static __device__ __forceinline__ u32 cvtpk(float lo, float hi) { return __builtin_bit_cast(u32, __builtin_convertvector(f32x2{lo, hi}, bf16x2_t)); }
static __device__ __forceinline__ float lo2f(u32 v) { return __uint_as_float(v << 16); }
static __device__ __forceinline__ float hi2f(u32 v) { return __uint_as_float(v & 0xffff0000u); }
template <int CTRL, int RM = 0xf> static __device__ __forceinline__ u32 dpp0(u32 x) { return (u32)__builtin_amdgcn_update_dpp(0, (int)x, CTRL, RM, 0xf, true); }
static __device__ __forceinline__ u32 wave_incl_scan(u32 x) {
  x += dpp0<0x111>(x); x += dpp0<0x112>(x); x += dpp0<0x114>(x); x += dpp0<0x118>(x);
  x += dpp0<0x142, 0xa>(x); x += dpp0<0x143, 0xc>(x); return x; }
#define MFMA(a, b, c) __builtin_amdgcn_mfma_f32_32x32x16_bf16(a, b, c, 0, 0, 0)
#define SB() __builtin_amdgcn_sched_barrier(0)
#define LBAR() asm volatile("s_waitcnt lgkmcnt(0)\n\ts_barrier" ::: "memory")
template <class T> static __device__ __forceinline__ T ldl(u32 a) { return *(LAS const T*)(size_t)a; }
template <class T> static __device__ __forceinline__ void stl(u32 a, T v) { *(LAS T*)(size_t)a = v; }
static __device__ __forceinline__ s16x4 ldtr(u32 a) { typedef short v4 __attribute__((ext_vector_type(4)));
  return __builtin_bit_cast(s16x4, __builtin_amdgcn_ds_read_tr16_b64_v4i16((LAS v4*)(size_t)a)); }
static __device__ __forceinline__ float silu_f(float z) { return z * __builtin_amdgcn_rcpf(1.f + __builtin_amdgcn_exp2f(-LOG2E * z)); }

#define STAMP(i)
#define STAMP_DECL
#define STAMP_DUMP(base, idx)
__global__ void __launch_bounds__(1024) k1_partition(const int* __restrict__ dst, const int* __restrict__ src, const u16* __restrict__ ea,
                                                    const u16* __restrict__ we, const u16* __restrict__ wh, const u16* __restrict__ pos, const u16* __restrict__ h,
                                                    char* __restrict__ ws, int N, int E, int CS) {
  __shared__ u32x2 srec[4096];
  __shared__ __attribute__((aligned(16))) u32 cnt[NBK];
  __shared__ __attribute__((aligned(16))) u32 start[NBK];
  const int tid = threadIdx.x, c = blockIdx.x;
  STAMP_DECL STAMP(0);
  const int e0 = c * CS + 4 * tid;
  const bool act = 4 * tid < CS && e0 < E;
  u32x4 d4 = {0, 0, 0, 0}, s4 = {0, 0, 0, 0}; u32x2 a4 = {0, 0};
  if (act) { d4 = *(const u32x4*)(dst + e0); s4 = *(const u32x4*)(src + e0); a4 = *(const u32x2*)(ea + e0); }
  const int pn = c * NPB + tid - (1024 - NPB);
  const bool pact = tid >= 1024 - NPB && pn < N;
  u32 pv[3] = {0, 0, 0};
  if (pact) { pv[0] = pos[pn * 3]; pv[1] = pos[pn * 3 + 1]; pv[2] = pos[pn * 3 + 2]; }
  const bool wact = c < 32 && tid >= 960;
  u32 wv[8] = {0, 0, 0, 0, 0, 0, 0, 0};
  if (wact) {
    const int l = tid - 960, g = c & 15;
    const u16* w = (c >= 16 ? wh : we) + 32 * (g & 1) + (l & 31) + (16 * (g >> 1) + 8 * (l >> 5)) * 64;
#pragma unroll
    for (int j = 0; j < 8; ++j) wv[j] = w[j * 64];
  }
  if (tid < NBK) cnt[tid] = 0;
  __syncthreads();
  asm volatile("" : "+v"(pv[0]), "+v"(pv[1]), "+v"(pv[2]), "+v"(wv[0]), "+v"(wv[1]), "+v"(wv[2]), "+v"(wv[3]), "+v"(wv[4]), "+v"(wv[5]), "+v"(wv[6]), "+v"(wv[7]));
  STAMP(1);
  u32 b[4], r[4];
  if (act) {
#pragma unroll
    for (int j = 0; j < 4; ++j) { b[j] = d4[j] / (u32)NPB; r[j] = atomicAdd(&cnt[b[j]], 1u); }
  }
  __syncthreads();
  STAMP(2);
  if (tid < 64) {
    const u32x4 cv = *(const u32x4*)&cnt[4 * tid];
    const u32 s1 = cv.x + cv.y, s2 = s1 + cv.z, s3 = s2 + cv.w;
    const u32 ex = wave_incl_scan(s3) - s3;
    u32x4 st = {ex, ex + cv.x, ex + s1, ex + s2};
    *(u32x4*)&start[4 * tid] = st;
    u32x4 t = {st.x | cv.x << 16, st.y | cv.y << 16, st.z | cv.z << 16, st.w | cv.w << 16};
    *(u32x4*)(ws + W_TAB + ((size_t)c * NBK + 4 * tid) * 4) = t;
  }
  __syncthreads();
  STAMP(3);
  if (act) {
#pragma unroll
    for (int j = 0; j < 4; ++j) {
      const u32 eav = (j & 1) ? (a4[j >> 1] & 0xffff0000u) : (a4[j >> 1] << 16);
      u32x2 rec = {s4[j] | (d4[j] - b[j] * NPB) << 16, eav};
      srec[start[b[j]] + r[j]] = rec;
    }
  }
  __syncthreads();
  STAMP(4);
  const int n = min(CS, E - c * CS);
  u32x4* o = (u32x4*)(ws + W_REC + (size_t)c * CS * 8);
  for (int i = tid; 2 * i < n; i += 1024) o[i] = *(const u32x4*)&srec[2 * i];
  if (pact) *(u32x2*)(ws + W_P8 + (size_t)pn * 8) = u32x2{pv[0] | pv[1] << 16, pv[2]};
  if (wact) {
    const int l = tid - 960;
    float v[8];
#pragma unroll
    for (int j = 0; j < 8; ++j) v[j] = bf2f(wv[j]) * ((FOLD && c < 16) ? -LOG2E : 1.f);
    *(u32x4*)(ws + W_WE + (size_t)(c * 64 + l) * 16) = u32x4{cvtpk(v[0], v[1]), cvtpk(v[2], v[3]), cvtpk(v[4], v[5]), cvtpk(v[6], v[7])};
  }
  STAMP(5);
  STAMP_DUMP(128u << 20, c * 16 + (tid >> 6));
}

struct Dma { const char* b0; const char* b1; const char* b2; const char* b3; };
static __device__ __forceinline__ void dma4(u32 v0, u32 v1, u32 v2, u32 v3, const Dma& d, u32 lds) {
  u32 sv;
  asm volatile("s_mov_b32 %0, m0\n\ts_mov_b32 m0, %9\n\ts_nop 0\n\t"
               "global_load_lds_dwordx4 %1, %5\n\t"
               "global_load_lds_dwordx4 %2, %6 offset:1024\n\t"
               "global_load_lds_dwordx4 %3, %7 offset:2048\n\t"
               "global_load_lds_dwordx4 %4, %8 offset:3072\n\t"
               "s_mov_b32 m0, %0"
               : "=&s"(sv) : "v"(v0), "v"(v1), "v"(v2), "v"(v3), "s"(d.b0), "s"(d.b1), "s"(d.b2), "s"(d.b3), "s"(lds) : "memory");
}

static __device__ __forceinline__ void slow_bucket(const u16* __restrict__ h, const u16* __restrict__ pos, const u16* __restrict__ we, const u16* __restrict__ wx,
                                                const u16* __restrict__ wh, const char* __restrict__ ws, u16* __restrict__ out, int N, int CS, int bk, u32 l0) {
  const int tid = threadIdx.x, lane = tid & 63, w = tid >> 6, n0 = bk * NPB;
  LAS float* macc = (LAS float*)(size_t)(l0 + L_STG);
  LAS float* pacc = (LAS float*)(size_t)(l0 + L_PA);
  __syncthreads();
  for (int i = tid; i < NPB * 64; i += 512) macc[i] = 0.f;
  for (int i = tid; i < NPB * 4; i += 512) pacc[i] = 0.f;
  __syncthreads();
  const float wxf = bf2f(wx[lane]);
  for (int c = w; c < NCH; c += 8) {
    const u32 ent = *(const u32*)(ws + W_TAB + ((size_t)c * NBK + bk) * 4);
    const int st = ent & 0xffff, cn = ent >> 16;
    for (int j = 0; j < cn; ++j) {
      const u32x2 rec = *(const u32x2*)(ws + W_REC + ((size_t)c * CS + st + j) * 8);
      const int s = rec.x & 0xffff, dl = rec.x >> 16, d = n0 + dl;
      float df[3], d2 = 0.f;
#pragma unroll
      for (int k = 0; k < 3; ++k) { df[k] = lo2f(cvtpk(bf2f(pos[d * 3 + k]) - bf2f(pos[s * 3 + k]), 0.f)); d2 += df[k] * df[k]; }
      d2 = lo2f(cvtpk(d2, 0.f));
      const float hd = bf2f(h[(size_t)d * 64 + lane]), hs = bf2f(h[(size_t)s * 64 + lane]);
      float z = 0.f;
#pragma unroll 4
      for (int k = 0; k < 64; ++k) z += __shfl(hd, k) * bf2f(we[k * 64 + lane]) + __shfl(hs, k) * bf2f(we[(64 + k) * 64 + lane]);
      z += d2 * bf2f(we[128 * 64 + lane]) + hi2f(rec.y) * bf2f(we[129 * 64 + lane]);
      const float m = lo2f(cvtpk(silu_f(z), 0.f));
      float cs = m * wxf;
#pragma unroll
      for (int o = 32; o > 0; o >>= 1) cs += __shfl_xor(cs, o);
      atomicAdd((float*)&macc[dl * 64 + lane], m);
      if (lane < 3) atomicAdd((float*)&pacc[dl * 4 + lane], (lane == 0 ? df[0] : lane == 1 ? df[1] : df[2]) * cs);
    }
  }
  __syncthreads();
  for (int nl = w; nl < NPB; nl += 8) {
    const int n = n0 + nl; if (n >= N) break;
    const float hv = bf2f(h[(size_t)n * 64 + lane]), mv = lo2f(cvtpk(macc[nl * 64 + lane], 0.f));
    float z = 0.f;
#pragma unroll 4
    for (int k = 0; k < 64; ++k) z += __shfl(hv, k) * bf2f(wh[k * 64 + lane]) + __shfl(mv, k) * bf2f(wh[(64 + k) * 64 + lane]);
    out[(size_t)n * 64 + lane] = (u16)cvtpk(hv + silu_f(z), 0.f);
    if (lane < 3) out[(size_t)N * 64 + (size_t)n * 3 + lane] = (u16)cvtpk(bf2f(pos[n * 3 + lane]) + pacc[nl * 4 + lane], 0.f);
  }
}

static __device__ __forceinline__ u32 mad16lo(u32 a, u32 b, u32 c) { u32 r; asm("v_mad_u32_u16 %0, %1, %2, %3" : "=v"(r) : "v"(a), "s"(b), "v"(c)); return r; }
static __device__ __forceinline__ u32 mad16hi(u32 a, u32 b, u32 c) { u32 r; asm("v_mad_u32_u16 %0, %1, %2, %3 op_sel:[1,0,0,0]" : "=v"(r) : "v"(a), "s"(b), "v"(c)); return r; }
static __device__ __forceinline__ bf16x8 cat8(s16x4 a, s16x4 b) { return __builtin_shufflevector(a, b, 0, 1, 2, 3, 4, 5, 6, 7); }
extern "C" __device__ float llvm_fmul_legacy(float, float) __asm("llvm.amdgcn.fmul.legacy");
static __device__ __forceinline__ float vmul(float a, float b) { return llvm_fmul_legacy(a, b); }
static __device__ __forceinline__ void wait_dma(u32 a, u32 b) {
  asm volatile("s_cmp_lt_u32 %0, %1\n\ts_cbranch_scc1 1f\n\ts_waitcnt vmcnt(0)\n1:\n\ts_waitcnt vmcnt(4)" :: "s"(a), "s"(b) : "scc", "memory"); }
static __device__ __forceinline__ void dma4_if(u32 a, u32 b, u32 v0, u32 v1, u32 v2, u32 v3, const Dma& d, u32 lds) {
  u32 sv;
  asm volatile("s_cmp_lt_u32 %10, %11\n\ts_cbranch_scc0 1f\n\t"
               "s_mov_b32 %0, m0\n\ts_mov_b32 m0, %9\n\ts_nop 0\n\t"
               "global_load_lds_dwordx4 %1, %5\n\t"
               "global_load_lds_dwordx4 %2, %6 offset:1024\n\t"
               "global_load_lds_dwordx4 %3, %7 offset:2048\n\t"
               "global_load_lds_dwordx4 %4, %8 offset:3072\n\t"
               "s_mov_b32 m0, %0\n1:"
               : "=&s"(sv) : "v"(v0), "v"(v1), "v"(v2), "v"(v3), "s"(d.b0), "s"(d.b1), "s"(d.b2), "s"(d.b3), "s"(lds), "s"(a), "s"(b) : "scc", "memory");
}
template <int V> struct IC { static constexpr int value = V; };

__global__ void __launch_bounds__(512, 2) k2_main(const u16* __restrict__ h, const u16* __restrict__ pos, const u16* __restrict__ we, const u16* __restrict__ wx,
                                                  const u16* __restrict__ wh, const char* __restrict__ ws, u16* __restrict__ out, int N, int CS) {
  extern __shared__ __attribute__((aligned(16))) char smem[];
  const u32 l0 = (u32)(size_t)(LAS char*)smem;
  const int tid = threadIdx.x, lane = tid & 63;
  const int w = __builtin_amdgcn_readfirstlane(tid >> 6);
  const int bk = (blockIdx.x & 7) * 32 + (blockIdx.x >> 3);
  const int n0 = bk * NPB;
  if (n0 >= N) return;
  STAMP_DECL STAMP(0);

  const int grp = lane >> 4, j16 = lane & 15, e32 = lane & 31, hh = lane >> 5;
  const int chunk0 = 32 * w + 8 * grp;
  const u32 ent = *(const u32*)(ws + W_TAB + ((size_t)(chunk0 + (j16 & 7)) * NBK + bk) * 4);
  const u32x2* p8 = (const u32x2*)(ws + W_P8);
  const int nmax = N - 1;
  u32x4 wt[2];
#pragma unroll
  for (int i = 0; i < 2; ++i) wt[i] = *(const u32x4*)(ws + W_WE + (size_t)(tid + 512 * i) * 16);
  bf16x8 hb[4];
  { const u16* hr = h + (size_t)min(n0 + 32 * w + e32, nmax) * 64 + 8 * hh;
#pragma unroll
    for (int s = 0; s < 4; ++s) hb[s] = *(const bf16x8*)(hr + 16 * s); }
  const u32x2 ptile = p8[min(n0 + tid, nmax)];
  const u32 wxr = wx[16 * ((tid >> 4) & 3) + (tid & 3) + 8 * ((tid >> 2) & 1) + 4 * ((tid >> 3) & 1)];
  const u32 wrr = we[128 * 64 + (tid & 127)];
  if (tid < 256) stl<u32>(l0 + L_CNT + 4 * tid, 0u);
  if (tid < 16) stl<u32>(l0 + L_MISC + 4 * tid, tid < 8 ? 0xffffffffu : 0u);
  if (tid < 128) stl<u32>(l0 + L_Z + 4 * tid, 0u);
  if (tid < NPB) stl<f32x4>(l0 + L_PA + 16 * tid, f32x4{0, 0, 0, 0});
  if (tid < 64) { const int u = tid < 36 ? tid : tid - 36, e = u >> 2, lim = tid < 36 ? 8 : 6;
    stl<u32>(l0 + L_OH + 4 * tid, (e < lim && (e >> 1) == (u & 3)) ? ((e & 1) ? 0x3f800000u : 0x3f80u) : 0u); }

  u32x2 rec[9]; u32 vmask = 0;
  u32 segb[8], segc[8];
  const u32 sb0 = (u32)chunk0 * (u32)CS + j16;
#define SEG_ENT(i) { const u32 e_ = dpp0<0x150 + i>(ent); segc[i] = e_ >> 16; segb[i] = sb0 + (u32)(i * CS) + (e_ & 0xffff); }
  SEG_ENT(0) SEG_ENT(1) SEG_ENT(2) SEG_ENT(3) SEG_ENT(4) SEG_ENT(5) SEG_ENT(6) SEG_ENT(7)
  const u32x2* recs = (const u32x2*)(ws + W_REC);
#pragma unroll
  for (int i = 0; i < 8; ++i) { const bool v = (u32)j16 < segc[i]; rec[i] = recs[v ? segb[i] : 0u]; vmask |= (v ? 1u : 0u) << i; }
  u32 novf;
  {
    u32 ex[8], tot = 0, mx = 0;
#pragma unroll
    for (int i = 0; i < 8; ++i) { ex[i] = segc[i] > 16 ? segc[i] - 16 : 0; tot += ex[i]; mx = max(mx, ex[i]); }
    u32 inc = tot; inc += dpp0<0x142, 0xa>(inc); inc += dpp0<0x143, 0xc>(inc);
    novf = __builtin_amdgcn_readlane(inc, 63);
    u32 gb = inc - tot + j16;
    const u32 lst = l0 + L_OV + 256 * w;
    if (__builtin_amdgcn_ballot_w64(mx > 16) == 0) {
#pragma unroll
      for (int i = 0; i < 8; ++i) { stl<u32>(((u32)j16 < ex[i] && gb < 64) ? lst + 4 * gb : l0 + L_DUM + 4 * lane, segb[i] + 16); gb += ex[i]; }
    } else {
#pragma unroll
      for (int i = 0; i < 8; ++i) { for (u32 k = 0; k + j16 < ex[i]; k += 16) if (gb + k < 64) stl<u32>(lst + 4 * (gb + k), segb[i] + 16 + k); gb += ex[i]; }
    }
    const bool v = (u32)lane < novf; const u32 idx = ldl<u32>(lst + 4 * lane); rec[8] = recs[v ? idx : 0u]; vmask |= (v ? 1u : 0u) << 8;
  }
  STAMP(1);
#pragma unroll
  for (int i = 0; i < 2; ++i) stl<u32x4>(l0 + L_STG + (tid + 512 * i) * 16, wt[i]);
  if (tid < NPB) stl<f32x4>(l0 + L_PT + 16 * tid, f32x4{lo2f(ptile.x), hi2f(ptile.x), lo2f(ptile.y), 0.f});
  if (tid < 64) stl<u16>(l0 + L_WX + 2 * tid, (u16)wxr);
  if (tid < 128) stl<u16>(l0 + L_WR + 2 * tid, (u16)cvtpk(bf2f(wrr) * -LOG2E, 0.f));
  LBAR();
  STAMP(2);
  if (novf > 64 && lane == 0) stl<u32>(l0 + L_MISC + 36, 1u);
  if (w < 7) {
    f32x16 A0, A1;
#pragma unroll
    for (int r = 0; r < 16; ++r) { A0[r] = 0.f; A1[r] = 0.f; }
#pragma unroll
    for (int s = 0; s < 4; ++s) {
      A0 = MFMA(ldl<bf16x8>(l0 + L_STG + (2 * s) * 1024 + lane * 16), hb[s], A0);
      A1 = MFMA(ldl<bf16x8>(l0 + L_STG + (2 * s + 1) * 1024 + lane * 16), hb[s], A1);
    }
    const u32 nl = 32 * w + e32;
    if (nl <= NPB) {
      const u32 a = l0 + L_HM + nl * RS + 8 * hh;
#pragma unroll
      for (int q = 0; q < 4; ++q) {
        stl<u32x2>(a + 16 * q, u32x2{cvtpk(A0[4 * q], A0[4 * q + 1]), cvtpk(A0[4 * q + 2], A0[4 * q + 3])});
        stl<u32x2>(a + 64 + 16 * q, u32x2{cvtpk(A1[4 * q], A1[4 * q + 1]), cvtpk(A1[4 * q + 2], A1[4 * q + 3])});
      }
    }
  }
  bf16x8 W[8];
#pragma unroll
  for (int i = 0; i < 8; ++i) W[i] = ldl<bf16x8>(l0 + L_STG + (8 + i) * 1024 + lane * 16);
  STAMP(3);
  u32 rank[9]; u32x2 ps[9];
#pragma unroll
  for (int i = 0; i < 9; ++i) {
    ps[i] = p8[rec[i].x & 0xffff];
    rank[i] = __hip_atomic_fetch_add((LAS u32*)(size_t)((vmask >> i & 1) ? l0 + L_CNT + 4 * (rec[i].x >> 16) : l0 + L_DUM + 4 * lane), 1u, __ATOMIC_RELAXED, __HIP_MEMORY_SCOPE_WORKGROUP);
  }
  STAMP(4);
  LBAR();
  STAMP(5);
  u32 wa0, wa1, wtb, wnt, wend; bool slow;
  {
    const u32x4 cv = ldl<u32x4>(l0 + L_CNT + 16 * lane);
    const u32 flag = ldl<u32>(l0 + L_MISC + 36);
    const u32 s1 = cv.x + cv.y, s2 = s1 + cv.z, s3 = s2 + cv.w;
    const u32 inc = wave_incl_scan(s3), ex = inc - s3;
    const u32 T = __builtin_amdgcn_readlane(inc, 63);
    const u32 off[4] = {ex, ex + cv.x, ex + s1, ex + s2};
    const float rT = T ? 8.f / (float)T : 0.f;
    u32 wv[4];
#pragma unroll
    for (int i = 0; i < 4; ++i) wv[i] = min(7u, (u32)((float)off[i] * rT));
    u32 prev = (u32)__builtin_amdgcn_update_dpp(-1, (int)wv[3], 0x138, 0xf, 0xf, false);
#pragma unroll
    for (int i = 0; i < 4; ++i) { const u32 n = 4 * lane + i;
      if (n < NPB && wv[i] != prev) stl<u32>(l0 + L_MISC + 4 * wv[i], off[i] << 8 | n);
      prev = wv[i]; }
    u32 cut = min(ldl<u32>(l0 + L_MISC + 4 * (lane & 7)), T << 8 | NPB);
    if (j16 >= 8) cut = T << 8 | NPB;
    cut = min(cut, (u32)__builtin_amdgcn_update_dpp(-1, (int)cut, 0x101, 0xf, 0xf, false));
    cut = min(cut, (u32)__builtin_amdgcn_update_dpp(-1, (int)cut, 0x102, 0xf, 0xf, false));
    cut = min(cut, (u32)__builtin_amdgcn_update_dpp(-1, (int)cut, 0x104, 0xf, 0xf, false));
    const u32 nxt = (u32)__builtin_amdgcn_update_dpp(-1, (int)cut, 0x101, 0xf, 0xf, false);
    u32 len = j16 < 8 ? (nxt >> 8) - (cut >> 8) : 0, nt = (len + 31) >> 5;
    u32 tb = nt; tb += dpp0<0x111>(tb); tb += dpp0<0x112>(tb); tb += dpp0<0x114>(tb);
    const u32 ttot = __builtin_amdgcn_readlane(tb, 7);
    tb -= nt;
    const u32 delta = 32 * tb - (cut >> 8);
    u32x4 op;
#pragma unroll
    for (int i = 0; i < 4; ++i) op[i] = off[i] + (u32)__builtin_amdgcn_ds_bpermute(4 * wv[i], (int)delta);
    stl<u32x4>(l0 + L_OFF + 16 * lane, op);
    wa0 = __builtin_amdgcn_readlane(cut, w) & 0xff; wa1 = __builtin_amdgcn_readlane(nxt, w) & 0xff;
    wtb = __builtin_amdgcn_readlane(tb, w); wnt = __builtin_amdgcn_readlane(nt, w);
    wend = 32 * wtb + __builtin_amdgcn_readlane(len, w);
    slow = ttot > MAXT || __builtin_amdgcn_readfirstlane(flag) != 0;
  }
  if (!slow) {
  STAMP(6);
  u32 pp[9];
#pragma unroll
  for (int i = 0; i < 9; ++i) pp[i] = ldl<u32>(l0 + L_OFF + 4 * (rec[i].x >> 16));
#pragma unroll
  for (int i = 0; i < 9; ++i) {
    const u32 p = (vmask >> i & 1) ? pp[i] + rank[i] : (u32)(MAXT * 32 + (lane & 31)); pp[i] = p;
    const u32 q = (p & ~31u) | (p & 7) << 2 | (p >> 3 & 3);
    stl<u16>(l0 + L_SR + 2 * q, (u16)rec[i].x); stl<unsigned char>(l0 + L_DS + q, (unsigned char)(rec[i].x >> 16));
  }
  {
    const u32 p = wend + lane;
    if (p < 32 * (wtb + wnt)) {
      const u32 q = (p & ~31u) | (p & 7) << 2 | (p >> 3 & 3);
      stl<u16>(l0 + L_SR + 2 * q, (u16)0); stl<unsigned char>(l0 + L_DS + q, (unsigned char)NPB);
      stl<u32x2>(l0 + L_V + 8 * p, u32x2{0, 0}); stl<u32>(l0 + L_DE + 4 * p, 0u);
    }
  }
  LBAR();
  STAMP(7);
  const Dma dh = {(const char*)h, (const char*)h - 1024, (const char*)h - 2048, (const char*)h - 3072};
  const u32 slot0 = l0 + L_STG + w * 8192;
  const u32 dch0 = 16 * ((lane & 7) ^ (lane >> 4)), dch1 = dch0 ^ 64;
  const u32 sraddr = l0 + L_SR + 64 * wtb + 8 * (lane >> 3);
  const u32 c128 = 128;
  auto issue = [&](u32 a, u32 b, u32x2 sv, u32 slot) {
    dma4_if(a, b, mad16lo(sv.x, c128, dch0), mad16hi(sv.x, c128, dch1), mad16lo(sv.y, c128, dch0), mad16hi(sv.y, c128, dch1), dh, slot);
  };
#pragma unroll
  for (int i = 0; i < 9; ++i) asm volatile("" : "+v"(ps[i].x), "+v"(ps[i].y));
  issue(0, wnt, ldl<u32x2>(sraddr), slot0);
  issue(1, wnt, ldl<u32x2>(sraddr + 64), slot0 + 4096);
  {
    f32x4 pd[9];
#pragma unroll
    for (int i = 0; i < 9; ++i) pd[i] = ldl<f32x4>(l0 + L_PT + 16 * (rec[i].x >> 16));
#pragma unroll
    for (int i = 0; i < 9; ++i) {
      const float dx = pd[i].x - lo2f(ps[i].x), dy = pd[i].y - hi2f(ps[i].x), dz = pd[i].z - lo2f(ps[i].y);
      const float d2 = dx * dx + dy * dy + dz * dz;
      stl<u32x2>(l0 + L_V + 8 * pp[i], u32x2{cvtpk(1.f, dx), cvtpk(dy, dz)});
      stl<u32>(l0 + L_DE + 4 * pp[i], (cvtpk(d2, 0.f) & 0xffffu) | rec[i].y);
    }
  }
  STAMP(8);
  LBAR();
  STAMP(9);

  const u32 swz = (e32 >> 1) & 7;
  u32 sa[4];
#pragma unroll
  for (int s = 0; s < 4; ++s) sa[s] = slot0 + e32 * 128 + 16 * ((2 * s + hh) ^ swz);
  const u32 eq = 4 * hh + ((lane >> 2) & 3), slotA = 4 * ((lane >> 4) & 1) + (lane & 3);
  u32 zv = l0 + L_Z; asm volatile("" : "+v"(zv));
  u32 a_dsb = l0 + L_DS + 32 * wtb + 4 * (e32 & 7) + (e32 >> 3);
  u32 a_dsw = l0 + L_DS + 32 * wtb + 4 * eq;
  u32 a_de = l0 + L_DE + 128 * wtb + 4 * e32;
  u32 a_v = l0 + L_V + 256 * wtb + 8 * eq;
  u32 a_sr = sraddr;
  const u32 ohb = l0 + L_OH + (hh ? 144 : 0), ohl = hh ? 6 : 8, nh8 = (u32)(-8 * hh), dem = hh ? 0xffffffffu : 0u;
  const u32 r4 = (lane >> 2) & 3, cofs = 32 * ((lane >> 4) & 1) + 8 * (lane & 3);
  const u32 pA = l0 + L_HM + (8 * hh + r4) * RS + cofs;
  const bool spc = hh && r4 >= 2;
  const u32 pB = spc ? l0 + L_WR + (r4 - 2) * 128 + cofs : pA + 4 * RS;
  u32 pM = spc ? 0 : 1; asm volatile("" : "+v"(pM));
  f32x16 Y0, Y1, X0, X1, ZERO;
#pragma unroll
  for (int r = 0; r < 16; ++r) { Y0[r] = 0.f; Y1[r] = 0.f; ZERO[r] = 0.f; }
  u32 base = wa0, tgt = wa0 + slotA;
  int bad = 0;
  const u32 fn = (lane & 31) >> 2, fi = lane & 3;
  u32 f_wx = l0 + L_WX + 16 * hh, f_m = l0 + L_HM + fn * RS + 8 * hh, f_p = l0 + L_PA + 16 * fn + 4 * (fi - 1);
  asm volatile("" : "+v"(f_wx), "+v"(f_m), "+v"(f_p));
  auto flush = [&]() {
    u32x4 c[4];
#pragma unroll
    for (int i = 0; i < 4; ++i) { c[0][i] = cvtpk(Y0[2 * i], Y0[2 * i + 1]); c[1][i] = cvtpk(Y0[8 + 2 * i], Y0[9 + 2 * i]);
                                  c[2][i] = cvtpk(Y1[2 * i], Y1[2 * i + 1]); c[3][i] = cvtpk(Y1[8 + 2 * i], Y1[9 + 2 * i]); }
    f32x16 D = ZERO;
#pragma unroll
    for (int i = 0; i < 4; ++i) D = MFMA(ldl<bf16x8>(f_wx + 32 * i), __builtin_bit_cast(bf16x8, c[i]), D);
    const u32 node = base + fn;
    if (node < wa1) {
      if (fi == 0) {
        const u32 a = f_m + base * RS;
#pragma unroll
        for (int q = 0; q < 4; ++q) {
          stl<u32x2>(a + 16 * q, u32x2{c[q >> 1][2 * (q & 1)], c[q >> 1][2 * (q & 1) + 1]});
          stl<u32x2>(a + 64 + 16 * q, u32x2{c[2 + (q >> 1)][2 * (q & 1)], c[2 + (q >> 1)][2 * (q & 1) + 1]});
        }
      } else if (hh == 0) stl<float>(f_p + 16 * base, D[0] * -LN2);
    }
  };
  if (wnt > 0) {
    bf16x8 As[4], Pb0, Pb1; u32x4 OH; u32x4 P0[2], P1[2];
    u32 lastc, lastn, aoh, aA, aB;
    u32 lastreal;
    { const u32 p = wend - 1, q = (p & ~31u) | (p & 7) << 2 | (p >> 3 & 3); lastreal = __builtin_amdgcn_readfirstlane((u32)ldl<unsigned char>(l0 + L_DS + q)); }
    float tt[4];
    auto front = [&](u32 dsb, u32 valid) {
      const u32 first = __builtin_amdgcn_readlane(dsb, 0); lastn = min((u32)__builtin_amdgcn_readlane(dsb, 31), lastreal);
      const u32 wb = min(first, (u32)(NPB - XW));
      bad = max(bad, (int)((lastn - wb) & valid));
      aoh = ohb + 16 * min(dsb - wb + nh8, ohl);
      const u32 wo = wb * RS;
      aA = pA + wo; aB = __umul24(pM, wo) + pB;
    };
    auto rdwin = [&]() { OH = ldl<u32x4>(aoh);
      Pb0 = cat8(ldtr(aA), ldtr(aB)); Pb1 = cat8(ldtr(aA + 64), ldtr(aB + 64)); };
#define EA(X, b) { _Pragma("unroll") for (int k_ = 0; k_ < 4; ++k_) tt[k_] = __builtin_amdgcn_exp2f(X[b + k_]); SB(); \
                   _Pragma("unroll") for (int k_ = 0; k_ < 4; ++k_) tt[k_] = 1.f + tt[k_]; SB(); }
#define RMC(X, b, P) { _Pragma("unroll") for (int k_ = 0; k_ < 4; ++k_) tt[k_] = __builtin_amdgcn_rcpf(tt[k_]); SB(); \
                   _Pragma("unroll") for (int k_ = 0; k_ < 4; ++k_) tt[k_] = vmul(tt[k_], X[b + k_]); SB(); \
                   P[(b) >> 3][((b) >> 1) & 3] = cvtpk(tt[0], tt[1]); P[(b) >> 3][(((b) >> 1) & 3) + 1] = cvtpk(tt[2], tt[3]); SB(); }
#define BF(x) __builtin_bit_cast(bf16x8, x)
    {
      const u32 dsb = ldl<unsigned char>(a_dsb), de = ldl<u32>(a_de); const u32x2 sv = ldl<u32x2>(a_sr + 128);
      wait_dma(1, wnt);
#pragma unroll
      for (int s = 0; s < 4; ++s) As[s] = ldl<bf16x8>(sa[s]);
      front(dsb, ~0u); lastc = lastn;
      rdwin();
      asm volatile("s_waitcnt lgkmcnt(0)" ::: "memory");
      issue(2, wnt, sv, slot0);
      OH.w |= de & dem;
      X0 = MFMA(As[0], W[0], ZERO); X0 = MFMA(As[1], W[2], X0); X0 = MFMA(As[2], W[4], X0); X0 = MFMA(As[3], W[6], X0); X0 = MFMA(BF(OH), Pb0, X0);
    }
    auto body = [&](auto uc, u32 t) {
      constexpr int U = decltype(uc)::value;
      const u32 dsw = ldl<u32>(a_dsw + 32 * U), dsb = ldl<unsigned char>(a_dsb + 32 * (U + 1)), de = ldl<u32>(a_de + 128 * (U + 1));
      const u32x2 sv = ldl<u32x2>(a_sr + 64 * (U + 3));
      SB();
      X1 = MFMA(BF(OH), Pb1, ZERO); SB();
      EA(X0, 0)
      X1 = MFMA(As[0], W[1], X1); SB();
      RMC(X0, 0, P0)
      X1 = MFMA(As[1], W[3], X1); SB();
      EA(X0, 4)
      X1 = MFMA(As[2], W[5], X1); SB();
      RMC(X0, 4, P0)
      X1 = MFMA(As[3], W[7], X1); SB();
      wait_dma(t + 2, wnt);
#pragma unroll
      for (int s = 0; s < 4; ++s) As[s] = ldl<bf16x8>(sa[s] + 4096 * (U ^ 1));
      SB();
      front(dsb, t + 1 < wnt ? ~0u : 0u);
      u32 sel[4];
#pragma unroll
      for (int a = 0; a < 4; ++a) sel[a] = ((dsw >> (8 * a)) & 0xff) == tgt ? a_v : zv;
      SB();
      EA(X0, 8) RMC(X0, 8, P0)
      asm volatile("s_waitcnt lgkmcnt(0)" ::: "memory");
      issue(t + 3, wnt, sv, slot0 + 4096 * (U ^ 1));
      rdwin();
      s16x4 Bq[4];
#pragma unroll
      for (int a = 0; a < 4; ++a) Bq[a] = ldtr(sel[a] + 64 * a + 256 * U);
      SB();
      EA(X0, 12) RMC(X0, 12, P0)
      X0 = MFMA(As[0], W[0], ZERO); SB();
      EA(X1, 0)
      X0 = MFMA(As[1], W[2], X0); SB();
      RMC(X1, 0, P1)
      X0 = MFMA(As[2], W[4], X0); SB();
      EA(X1, 4)
      X0 = MFMA(As[3], W[6], X0); SB();
      RMC(X1, 4, P1)
      OH.w |= de & dem; SB();
      X0 = MFMA(BF(OH), Pb0, X0); SB();
      EA(X1, 8)
      bf16x8 B0 = cat8(Bq[0], Bq[1]), B1 = cat8(Bq[2], Bq[3]);
      Y0 = MFMA(BF(P0[0]), B0, Y0); SB();
      RMC(X1, 8, P1)
      Y0 = MFMA(BF(P0[1]), B1, Y0); SB();
      EA(X1, 12)
      Y1 = MFMA(BF(P1[0]), B0, Y1); SB();
      RMC(X1, 12, P1)
      Y1 = MFMA(BF(P1[1]), B1, Y1); SB();
      if (lastc >= base + 8) do {
        flush(); base += 8; tgt += 8;
        asm volatile("" : "+s"(base), "+v"(tgt));
#pragma unroll
        for (int a = 0; a < 4; ++a) Bq[a] = ldtr((((dsw >> (8 * a)) & 0xff) == tgt ? a_v : zv) + 64 * a + 256 * U);
        B0 = cat8(Bq[0], Bq[1]); B1 = cat8(Bq[2], Bq[3]);
        Y0 = MFMA(BF(P0[0]), B0, ZERO); Y1 = MFMA(BF(P1[0]), B0, ZERO);
        Y0 = MFMA(BF(P0[1]), B1, Y0); Y1 = MFMA(BF(P1[1]), B1, Y1);
      } while (lastc >= base + 8);
      lastc = lastn;
    };
    for (u32 t = 0;; t += 2) {
      body(IC<0>{}, t);
      if (t + 1 >= wnt) break;
      body(IC<1>{}, t + 1);
      if (t + 2 >= wnt) break;
      a_dsb += 64; a_dsw += 64; a_de += 256; a_v += 512; a_sr += 128;
    }
  }
  STAMP(10);
  {
    const u32 rb = (u32)(n0 + 32 * w) + (lane >> 3), nm = (u32)N - 1;
    dma4(min(rb, nm) * 128 + dch0, min(rb + 8, nm) * 128 + dch1, min(rb + 16, nm) * 128 + dch0, min(rb + 24, nm) * 128 + dch1, dh, slot0);
    u32 sv; const char* g = ws + W_WH + w * 2048; const u32 vo = lane * 16;
    asm volatile("s_mov_b32 %0, m0\n\ts_mov_b32 m0, %3\n\ts_nop 0\n\tglobal_load_lds_dwordx4 %1, %2\n\tglobal_load_lds_dwordx4 %1, %2 offset:1024\n\ts_mov_b32 m0, %0"
                 : "=&s"(sv) : "v"(vo), "s"(g), "s"(slot0 + 4096) : "memory");
  }
  if (base < wa1) { flush(); base += 8; }
  if (base < wa1) {
#pragma unroll
    for (int r = 0; r < 16; ++r) { Y0[r] = 0.f; Y1[r] = 0.f; }
    do { flush(); base += 8; } while (base < wa1);
  }
  if (bad >= XW && lane == 0) stl<u32>(l0 + L_MISC + 36, 1u);
  STAMP(11);
  asm volatile("s_waitcnt vmcnt(0) lgkmcnt(0)\n\ts_barrier" ::: "memory");
  STAMP(12);
  slow = __builtin_amdgcn_readfirstlane(ldl<u32>(l0 + L_MISC + 36)) != 0;

  if (!slow) {
  if (w < 7) {
    const u32 nl = 32 * w + e32, mrow = l0 + L_HM + min(nl, (u32)NPB) * RS + 16 * hh;
    f32x16 A0, A1, C0, C1;
#pragma unroll
    for (int r = 0; r < 16; ++r) { A0[r] = 0.f; A1[r] = 0.f; C0[r] = 0.f; C1[r] = 0.f; }
#pragma unroll
    for (int s = 0; s < 4; ++s) {
      const bf16x8 hv = ldl<bf16x8>(sa[s]), mb = ldl<bf16x8>(mrow + 32 * s);
      const u32 wa = l0 + L_STG + 4096 + lane * 16;
      const bf16x8 w0 = ldl<bf16x8>(wa + s * 8192), w1 = ldl<bf16x8>(wa + s * 8192 + 1024);
      const bf16x8 w2 = ldl<bf16x8>(wa + (4 + s) * 8192), w3 = ldl<bf16x8>(wa + (4 + s) * 8192 + 1024);
      A0 = MFMA(w0, hv, A0); A1 = MFMA(w1, hv, A1); C0 = MFMA(w2, mb, C0); C1 = MFMA(w3, mb, C1);
    }
#pragma unroll
    for (int ft = 0; ft < 2; ++ft)
#pragma unroll
      for (int q = 0; q < 4; ++q) {
        const u32 a = slot0 + e32 * 128 + 16 * ((4 * ft + q) ^ swz) + 8 * hh;
        const u32x2 hv = ldl<u32x2>(a);
        float o[4];
#pragma unroll
        for (int k = 0; k < 4; ++k) { const float z = ft ? A1[4 * q + k] - LN2 * C1[4 * q + k] : A0[4 * q + k] - LN2 * C0[4 * q + k]; o[k] = silu_f(z); }
        stl<u32x2>(a, u32x2{cvtpk(lo2f(hv.x) + o[0], hi2f(hv.x) + o[1]), cvtpk(lo2f(hv.y) + o[2], hi2f(hv.y) + o[3])});
      }
    asm volatile("s_waitcnt lgkmcnt(0)" ::: "memory");
#pragma unroll
    for (int a = 0; a < 4; ++a) {
      const u32 row = 8 * a + (lane >> 3), nr = 32 * w + row;
      const u32x4 v = ldl<u32x4>(slot0 + a * 1024 + lane * 16);
      if (nr < NPB && n0 + nr < (u32)N) *(u32x4*)((char*)out + (size_t)(n0 + nr) * 128 + ((a & 1) ? dch1 : dch0)) = v;
    }
  } else {
    const int nn = min(NPB, N - n0);
    u32* po = (u32*)(out + (size_t)N * 64 + (size_t)n0 * 3);
    for (int i = lane; 2 * i < 3 * nn; i += 64) {
      const int e0 = 2 * i, e1 = e0 + 1, a0 = e0 / 3, a1 = e1 / 3, c0 = e0 - 3 * a0, c1 = e1 - 3 * a1;
      const float v0 = ldl<float>(l0 + L_PT + 16 * a0 + 4 * c0) + ldl<float>(l0 + L_PA + 16 * a0 + 4 * c0);
      const float v1 = ldl<float>(l0 + L_PT + 16 * a1 + 4 * c1) + ldl<float>(l0 + L_PA + 16 * a1 + 4 * c1);
      po[i] = cvtpk(v0, v1);
    }
  }
  STAMP(13);
  }
  }
  if (slow) slow_bucket(h, pos, we, wx, wh, ws, out, N, CS, bk, l0);
  STAMP_DUMP(160u << 20, bk * 8 + w);
}

__global__ void gen_zero(float* p, int n) { int i = blockIdx.x * blockDim.x + threadIdx.x; if (i < n) p[i] = 0.f; }
__global__ void gen_edge(const u16* h, const u16* pos, const u16* ea, const int* src, const int* dst, const u16* we, const u16* wx, float* mi, float* pd, int E) {
  const int lane = threadIdx.x & 63, w = threadIdx.x >> 6, nw = blockDim.x >> 6;
  const float wxf = bf2f(wx[lane]);
  for (int e = blockIdx.x * nw + w; e < E; e += gridDim.x * nw) {
    const int s = src[e], d = dst[e];
    float df[3], d2 = 0.f;
#pragma unroll
    for (int k = 0; k < 3; ++k) { df[k] = lo2f(cvtpk(bf2f(pos[(size_t)d * 3 + k]) - bf2f(pos[(size_t)s * 3 + k]), 0.f)); d2 += df[k] * df[k]; }
    d2 = lo2f(cvtpk(d2, 0.f));
    const float hd = bf2f(h[(size_t)d * 64 + lane]), hs = bf2f(h[(size_t)s * 64 + lane]);
    float z = 0.f;
#pragma unroll 4
    for (int k = 0; k < 64; ++k) z += __shfl(hd, k) * bf2f(we[k * 64 + lane]) + __shfl(hs, k) * bf2f(we[(64 + k) * 64 + lane]);
    z += d2 * bf2f(we[128 * 64 + lane]) + bf2f(ea[e]) * bf2f(we[129 * 64 + lane]);
    const float m = lo2f(cvtpk(silu_f(z), 0.f));
    float cs = m * wxf;
#pragma unroll
    for (int o = 32; o > 0; o >>= 1) cs += __shfl_xor(cs, o);
    atomicAdd(&mi[(size_t)d * 64 + lane], m);
    if (lane < 3) atomicAdd(&pd[(size_t)d * 3 + lane], (lane == 0 ? df[0] : lane == 1 ? df[1] : df[2]) * cs);
  }
}
__global__ void gen_node(const u16* h, const u16* pos, const u16* wh, const float* mi, const float* pd, u16* out, int N) {
  const int lane = threadIdx.x & 63, w = threadIdx.x >> 6, nw = blockDim.x >> 6;
  for (int n = blockIdx.x * nw + w; n < N; n += gridDim.x * nw) {
    const float hv = bf2f(h[(size_t)n * 64 + lane]), mv = lo2f(cvtpk(mi[(size_t)n * 64 + lane], 0.f));
    float z = 0.f;
#pragma unroll 4
    for (int k = 0; k < 64; ++k) z += __shfl(hv, k) * bf2f(wh[k * 64 + lane]) + __shfl(mv, k) * bf2f(wh[(64 + k) * 64 + lane]);
    out[(size_t)n * 64 + lane] = (u16)cvtpk(hv + silu_f(z), 0.f);
    if (lane < 3) out[(size_t)N * 64 + (size_t)n * 3 + lane] = (u16)cvtpk(bf2f(pos[(size_t)n * 3 + lane]) + pd[(size_t)n * 3 + lane], 0.f);
  }
}

extern "C" void kernel_launch(void* const* d_in, const int* in_sizes, int n_in, void* d_out, int out_size, void* d_ws, size_t ws_size, hipStream_t stream) {
  const u16* h = (const u16*)d_in[0];
  const u16* pos = (const u16*)d_in[1];
  const u16* ea = (const u16*)d_in[2];
  const int* src = (const int*)d_in[3];
  const int* dst = (const int*)d_in[4];
  const u16* we = (const u16*)d_in[5];
  const u16* wx = (const u16*)d_in[6];
  const u16* wh = (const u16*)d_in[7];
  const int N = in_sizes[0] / 64, E = in_sizes[3];
  const int CS = ((E + NCH - 1) / NCH + 3) & ~3;
  const bool fast = N >= 2 && N <= NPB * NBK && (N & 1) == 0 && (E & 3) == 0 && E > 0 && CS <= 4096 && W_REC + (size_t)NCH * CS * 8 <= ws_size;
  if (fast) {
    (void)hipFuncSetAttribute((const void*)k2_main, hipFuncAttributeMaxDynamicSharedMemorySize, L_END);
    k1_partition<<<NCH, 1024, 0, stream>>>(dst, src, ea, we, wh, pos, h, (char*)d_ws, N, E, CS);
    k2_main<<<NBK, 512, L_END, stream>>>(h, pos, we, wx, wh, (const char*)d_ws, (u16*)d_out, N, CS);
  } else {
    float* mi = (float*)d_ws; float* pd = mi + (size_t)N * 64;
    const int nz = N * 67;
    gen_zero<<<(nz + 255) / 256, 256, 0, stream>>>(mi, nz);
    gen_edge<<<2048, 256, 0, stream>>>(h, pos, ea, src, dst, we, wx, mi, pd, E);
    gen_node<<<1024, 256, 0, stream>>>(h, pos, wh, mi, pd, (u16*)d_out, N);
  }
}
```

```cpp
#include <hip/hip_runtime.h>
#include <hip/hip_bf16.h>
#include <stdint.h>

typedef unsigned short u16;
typedef unsigned u32;
typedef short bf16x8 __attribute__((ext_vector_type(8)));
typedef short s16x4 __attribute__((ext_vector_type(4)));
typedef float f32x16 __attribute__((ext_vector_type(16)));
typedef float f32x4 __attribute__((ext_vector_type(4)));
typedef unsigned u32x4 __attribute__((ext_vector_type(4)));
typedef unsigned u32x2 __attribute__((ext_vector_type(2)));
#define LAS __attribute__((address_space(3)))

constexpr int NPB = 196;
constexpr int NBK = 256;
constexpr int NCH = 256;
constexpr int MAXT = 112;
constexpr float LOG2E = 1.4426950408889634f, LN2 = 0.6931471805599453f;

constexpr int NT1 = MAXT + 1;
constexpr int RS = 144;
constexpr int XW = 14;
constexpr int L_STG = 0;
constexpr int L_V = 65536;
constexpr int L_DE = L_V + NT1 * 32 * 8;
constexpr int L_SR = L_DE + NT1 * 32 * 4;
constexpr int L_DS = L_SR + NT1 * 32 * 2;
constexpr int L_HM = L_DS + NT1 * 32;
constexpr int L_PT = L_HM + 197 * RS;
constexpr int L_PA = L_PT + NPB * 16;
constexpr int L_CNT = L_PA + NPB * 16;
constexpr int L_OFF = L_CNT + 1024;
constexpr int L_WX = L_OFF + 1024;
constexpr int L_WR = L_WX + 128;
constexpr int L_OH = L_WR + 256;
constexpr int L_Z = L_OH + 256;
constexpr int L_DUM = L_Z + 512;
constexpr int L_MISC = L_DUM + 256;
constexpr int L_OV = L_MISC + 64;
constexpr int L_END = L_OV + 8 * 256;
constexpr int L_SREC = L_STG;
constexpr int L_WRAW = L_STG + 32768;
constexpr int L_HC = L_STG + 49152;
constexpr int L_HS = L_HC + 1024;
static_assert(L_HM % 16 == 0 && L_PT % 16 == 0 && L_CNT % 16 == 0 && L_Z % 16 == 0 && L_OV % 16 == 0 && L_WX % 16 == 0 && L_OH % 16 == 0, "align");
static_assert(L_END <= 160 * 1024, "lds");

constexpr size_t W_EP = 0;
constexpr size_t W_TAB = 4096;
constexpr size_t W_REC = W_TAB + NCH * NBK * 8;

static __device__ __forceinline__ float bf2f(u32 v) { return __uint_as_float(v << 16); }
typedef __bf16 bf16x2_t __attribute__((ext_vector_type(2)));
typedef float f32x2 __attribute__((ext_vector_type(2)));
static __device__ __forceinline__ u32 cvtpk(float lo, float hi) { return __builtin_bit_cast(u32, __builtin_convertvector(f32x2{lo, hi}, bf16x2_t)); }
static __device__ __forceinline__ float lo2f(u32 v) { return __uint_as_float(v << 16); }
static __device__ __forceinline__ float hi2f(u32 v) { return __uint_as_float(v & 0xffff0000u); }
template <int CTRL, int RM = 0xf> static __device__ __forceinline__ u32 dpp0(u32 x) { return (u32)__builtin_amdgcn_update_dpp(0, (int)x, CTRL, RM, 0xf, true); }
static __device__ __forceinline__ u32 wave_incl_scan(u32 x) {
  x += dpp0<0x111>(x); x += dpp0<0x112>(x); x += dpp0<0x114>(x); x += dpp0<0x118>(x);
  x += dpp0<0x142, 0xa>(x); x += dpp0<0x143, 0xc>(x); return x; }
#define MFMA(a, b, c) __builtin_amdgcn_mfma_f32_32x32x16_bf16(a, b, c, 0, 0, 0)
#define SB() __builtin_amdgcn_sched_barrier(0)
#define LBAR() asm volatile("s_waitcnt lgkmcnt(0)\n\ts_barrier" ::: "memory")
template <class T> static __device__ __forceinline__ T ldl(u32 a) { return *(LAS const T*)(size_t)a; }
template <class T> static __device__ __forceinline__ void stl(u32 a, T v) { *(LAS T*)(size_t)a = v; }
static __device__ __forceinline__ s16x4 ldtr(u32 a) { typedef short v4 __attribute__((ext_vector_type(4)));
  return __builtin_bit_cast(s16x4, __builtin_amdgcn_ds_read_tr16_b64_v4i16((LAS v4*)(size_t)a)); }
static __device__ __forceinline__ float silu_f(float z) { return z * __builtin_amdgcn_rcpf(1.f + __builtin_amdgcn_exp2f(-LOG2E * z)); }

#define STAMP(i)
#define STAMP_DECL
#define STAMP_DUMP(base, idx)
struct Dma { const char* b0; const char* b1; const char* b2; const char* b3; };
static __device__ __forceinline__ void dma4(u32 v0, u32 v1, u32 v2, u32 v3, const Dma& d, u32 lds) {
  u32 sv;
  asm volatile("s_mov_b32 %0, m0\n\ts_mov_b32 m0, %9\n\ts_nop 0\n\t"
               "global_load_lds_dwordx4 %1, %5\n\t"
               "global_load_lds_dwordx4 %2, %6 offset:1024\n\t"
               "global_load_lds_dwordx4 %3, %7 offset:2048\n\t"
               "global_load_lds_dwordx4 %4, %8 offset:3072\n\t"
               "s_mov_b32 m0, %0"
               : "=&s"(sv) : "v"(v0), "v"(v1), "v"(v2), "v"(v3), "s"(d.b0), "s"(d.b1), "s"(d.b2), "s"(d.b3), "s"(lds) : "memory");
}

static __device__ __forceinline__ void slow_bucket(const u16* __restrict__ h, const u16* __restrict__ pos, const u16* __restrict__ ea, const int* __restrict__ src,
                                                const int* __restrict__ dst, const u16* __restrict__ we, const u16* __restrict__ wx,
                                                const u16* __restrict__ wh, u16* __restrict__ out, int N, int E, int bk, u32 l0) {
  const int tid = threadIdx.x, lane = tid & 63, w = tid >> 6, n0 = bk * NPB;
  LAS float* macc = (LAS float*)(size_t)(l0 + L_STG);
  LAS float* pacc = (LAS float*)(size_t)(l0 + L_PA);
  __syncthreads();
  for (int i = tid; i < NPB * 64; i += 512) macc[i] = 0.f;
  for (int i = tid; i < NPB * 4; i += 512) pacc[i] = 0.f;
  __syncthreads();
  const float wxf = bf2f(wx[lane]);
  for (int eb = 64 * w; eb < E; eb += 512) {
    const int dv = eb + lane < E ? dst[eb + lane] : -1;
    unsigned long long m = __builtin_amdgcn_ballot_w64((u32)(dv - n0) < (u32)NPB && dv >= 0);
    while (m) {
      const int l = __builtin_ctzll(m); m &= m - 1;
      const int e = eb + l, d = __builtin_amdgcn_readlane(dv, l), s = src[e], dl = d - n0;
      float df[3], d2 = 0.f;
#pragma unroll
      for (int k = 0; k < 3; ++k) { df[k] = lo2f(cvtpk(bf2f(pos[d * 3 + k]) - bf2f(pos[s * 3 + k]), 0.f)); d2 += df[k] * df[k]; }
      d2 = lo2f(cvtpk(d2, 0.f));
      const float hd = bf2f(h[(size_t)d * 64 + lane]), hs = bf2f(h[(size_t)s * 64 + lane]);
      float z = 0.f;
#pragma unroll 4
      for (int k = 0; k < 64; ++k) z += __shfl(hd, k) * bf2f(we[k * 64 + lane]) + __shfl(hs, k) * bf2f(we[(64 + k) * 64 + lane]);
      z += d2 * bf2f(we[128 * 64 + lane]) + bf2f(ea[e]) * bf2f(we[129 * 64 + lane]);
      const float mm = lo2f(cvtpk(silu_f(z), 0.f));
      float cs = mm * wxf;
#pragma unroll
      for (int o = 32; o > 0; o >>= 1) cs += __shfl_xor(cs, o);
      atomicAdd((float*)&macc[dl * 64 + lane], mm);
      if (lane < 3) atomicAdd((float*)&pacc[dl * 4 + lane], (lane == 0 ? df[0] : lane == 1 ? df[1] : df[2]) * cs);
    }
  }
  __syncthreads();
  for (int nl = w; nl < NPB; nl += 8) {
    const int n = n0 + nl; if (n >= N) break;
    const float hv = bf2f(h[(size_t)n * 64 + lane]), mv = lo2f(cvtpk(macc[nl * 64 + lane], 0.f));
    float z = 0.f;
#pragma unroll 4
    for (int k = 0; k < 64; ++k) z += __shfl(hv, k) * bf2f(wh[k * 64 + lane]) + __shfl(mv, k) * bf2f(wh[(64 + k) * 64 + lane]);
    out[(size_t)n * 64 + lane] = (u16)cvtpk(hv + silu_f(z), 0.f);
    if (lane < 3) out[(size_t)N * 64 + (size_t)n * 3 + lane] = (u16)cvtpk(bf2f(pos[n * 3 + lane]) + pacc[nl * 4 + lane], 0.f);
  }
}
static __device__ __forceinline__ u32 umul24(u32 a, u32 b) { return (u32)__umul24(a, b); }
static __device__ __forceinline__ u32 mad16lo(u32 a, u32 b, u32 c) { u32 r; asm("v_mad_u32_u16 %0, %1, %2, %3" : "=v"(r) : "v"(a), "s"(b), "v"(c)); return r; }
static __device__ __forceinline__ u32 mad16hi(u32 a, u32 b, u32 c) { u32 r; asm("v_mad_u32_u16 %0, %1, %2, %3 op_sel:[1,0,0,0]" : "=v"(r) : "v"(a), "s"(b), "v"(c)); return r; }
static __device__ __forceinline__ bf16x8 cat8(s16x4 a, s16x4 b) { return __builtin_shufflevector(a, b, 0, 1, 2, 3, 4, 5, 6, 7); }
extern "C" __device__ float llvm_fmul_legacy(float, float) __asm("llvm.amdgcn.fmul.legacy");
static __device__ __forceinline__ float vmul(float a, float b) { return llvm_fmul_legacy(a, b); }
static __device__ __forceinline__ void wait_dma(u32 a, u32 b) {
  asm volatile("s_cmp_lt_u32 %0, %1\n\ts_cbranch_scc1 1f\n\ts_waitcnt vmcnt(0)\n1:\n\ts_waitcnt vmcnt(4)" :: "s"(a), "s"(b) : "scc", "memory"); }
static __device__ __forceinline__ void dma4_if(u32 a, u32 b, u32 v0, u32 v1, u32 v2, u32 v3, const Dma& d, u32 lds) {
  u32 sv;
  asm volatile("s_cmp_lt_u32 %10, %11\n\ts_cbranch_scc0 1f\n\t"
               "s_mov_b32 %0, m0\n\ts_mov_b32 m0, %9\n\ts_nop 0\n\t"
               "global_load_lds_dwordx4 %1, %5\n\t"
               "global_load_lds_dwordx4 %2, %6 offset:1024\n\t"
               "global_load_lds_dwordx4 %3, %7 offset:2048\n\t"
               "global_load_lds_dwordx4 %4, %8 offset:3072\n\t"
               "s_mov_b32 m0, %0\n1:"
               : "=&s"(sv) : "v"(v0), "v"(v1), "v"(v2), "v"(v3), "s"(d.b0), "s"(d.b1), "s"(d.b2), "s"(d.b3), "s"(lds), "s"(a), "s"(b) : "scc", "memory");
}
template <int V> struct IC { static constexpr int value = V; };

constexpr u32 SPIN_MAX = 1u << 15;
typedef __attribute__((address_space(1))) unsigned long long gu64;

__global__ void __launch_bounds__(512, 2) egnn_fused(const u16* __restrict__ h, const u16* __restrict__ pos, const u16* __restrict__ ea, const int* __restrict__ src,
                                                     const int* __restrict__ dst, const u16* __restrict__ we, const u16* __restrict__ wx, const u16* __restrict__ wh,
                                                     char* __restrict__ ws, u16* __restrict__ out, int N, int E, int CS) {
  extern __shared__ __attribute__((aligned(16))) char smem[];
  const u32 l0 = (u32)(size_t)(LAS char*)smem;
  const int tid = threadIdx.x, lane = tid & 63;
  const int w = __builtin_amdgcn_readfirstlane(tid >> 6);
  const int c = blockIdx.x;
  const int bk = (blockIdx.x & 7) * 32 + (blockIdx.x >> 3);
  const int n0 = bk * NPB;
  STAMP_DECL STAMP(0);

  const int grp = lane >> 4, j16 = lane & 15, e32 = lane & 31, hh = lane >> 5;
  const int e0 = c * CS + 4 * tid;
  const bool act0 = 4 * tid < CS && e0 < E, act1 = 4 * tid + 2048 < CS && e0 + 2048 < E;
  const __amdgpu_buffer_rsrc_t rdst = __builtin_amdgcn_make_buffer_rsrc((void*)dst, 0, E * 4, 0x00020000), rsrc_ = __builtin_amdgcn_make_buffer_rsrc((void*)src, 0, E * 4, 0x00020000);
  const __amdgpu_buffer_rsrc_t rea = __builtin_amdgcn_make_buffer_rsrc((void*)ea, 0, E * 2, 0x00020000);
  const u32 eo0 = act0 ? (u32)e0 : 0x1fffff00u, eo1 = act1 ? (u32)e0 + 2048 : 0x1fffff00u;
  u32x4 d4[2], s4[2]; u32x2 a4[2];
  d4[0] = __builtin_bit_cast(u32x4, __builtin_amdgcn_raw_buffer_load_b128(rdst, eo0 * 4, 0, 0));
  d4[1] = __builtin_bit_cast(u32x4, __builtin_amdgcn_raw_buffer_load_b128(rdst, eo1 * 4, 0, 0));
  s4[0] = __builtin_bit_cast(u32x4, __builtin_amdgcn_raw_buffer_load_b128(rsrc_, eo0 * 4, 0, 0));
  s4[1] = __builtin_bit_cast(u32x4, __builtin_amdgcn_raw_buffer_load_b128(rsrc_, eo1 * 4, 0, 0));
  a4[0] = __builtin_bit_cast(u32x2, __builtin_amdgcn_raw_buffer_load_b64(rea, eo0 * 2, 0, 0));
  a4[1] = __builtin_bit_cast(u32x2, __builtin_amdgcn_raw_buffer_load_b64(rea, eo1 * 2, 0, 0));
  const unsigned long long epv = __hip_atomic_load((gu64*)(ws + W_EP), __ATOMIC_RELAXED, __HIP_MEMORY_SCOPE_AGENT);
  u32x4 wt[2];
#pragma unroll
  for (int i = 0; i < 2; ++i) wt[i] = ((const u32x4*)we)[tid + 512 * i];
  const u32 wxr = wx[16 * ((tid >> 4) & 3) + (tid & 3) + 8 * ((tid >> 2) & 1) + 4 * ((tid >> 3) & 1)];
  const u32 wrr = we[128 * 64 + (tid & 127)];
  if (tid < 256) { stl<u32>(l0 + L_CNT + 4 * tid, 0u); stl<u32>(l0 + L_HC + 4 * tid, 0u); }
  if (tid < 16) stl<u32>(l0 + L_MISC + 4 * tid, tid < 8 ? 0xffffffffu : 0u);
  if (tid < 128) stl<u32>(l0 + L_Z + 4 * tid, 0u);
  if (tid < NPB) stl<f32x4>(l0 + L_PA + 16 * tid, f32x4{0, 0, 0, 0});
  if (tid < 64) { const int u = tid < 36 ? tid : tid - 36, e = u >> 2, lim = tid < 36 ? 8 : 6;
    stl<u32>(l0 + L_OH + 4 * tid, (e < lim && (e >> 1) == (u & 3)) ? ((e & 1) ? 0x3f800000u : 0x3f80u) : 0u); }
  LBAR();
  STAMP(1);
  asm volatile("" :: "v"(d4[0]), "v"(d4[1]) : "memory");
  STAMP(2);
  const int nmax = N - 1;
  const __amdgpu_buffer_rsrc_t rpos = __builtin_amdgcn_make_buffer_rsrc((void*)pos, 0, N * 6, 0x00020000);
  bf16x8 hb[4];
  { const u16* hr = h + (size_t)min(n0 + 32 * w + e32, nmax) * 64 + 8 * hh;
#pragma unroll
    for (int s = 0; s < 4; ++s) hb[s] = *(const bf16x8*)(hr + 16 * s); }
  const u32 ptn = min(n0 + tid, nmax);
  const u32x2 ptile = __builtin_bit_cast(u32x2, __builtin_amdgcn_raw_buffer_load_b64(rpos, (ptn * 6) & ~3u, 0, 0));
  u32 b[8], r[8];
#pragma unroll
  for (int j = 0; j < 8; ++j) b[j] = umul24(d4[j >> 2][j & 3], 85599u) >> 24;
  if (act0) {
#pragma unroll
    for (int j = 0; j < 4; ++j) r[j] = __hip_atomic_fetch_add((LAS u32*)(size_t)(l0 + L_HC + 4 * b[j]), 1u, __ATOMIC_RELAXED, __HIP_MEMORY_SCOPE_WORKGROUP);
  }
  if (act1) {
#pragma unroll
    for (int j = 4; j < 8; ++j) r[j] = __hip_atomic_fetch_add((LAS u32*)(size_t)(l0 + L_HC + 4 * b[j]), 1u, __ATOMIC_RELAXED, __HIP_MEMORY_SCOPE_WORKGROUP);
  }
#pragma unroll
  for (int i = 0; i < 2; ++i) {
    const u32 q = tid + 512 * i;
    u32x4 o;
#pragma unroll
    for (int k = 0; k < 4; ++k) o[k] = cvtpk(lo2f(wt[i][k]) * -LOG2E, hi2f(wt[i][k]) * -LOG2E);
    stl<u32x4>(l0 + L_WRAW + ((q * 16) ^ ((q >> 4 & 1) << 6)), o);
  }
  if (tid < 64) stl<u16>(l0 + L_WX + 2 * tid, (u16)wxr);
  if (tid < 128) stl<u16>(l0 + L_WR + 2 * tid, (u16)cvtpk(bf2f(wrr) * -LOG2E, 0.f));
  LBAR();
  STAMP(3);
  u32x4 tabv;
  {
    const u32x4 cv = ldl<u32x4>(l0 + L_HC + 16 * lane);
    const u32 s1 = cv.x + cv.y, s2 = s1 + cv.z, s3 = s2 + cv.w;
    const u32 ex = wave_incl_scan(s3) - s3;
    const u32x4 st = {ex, ex + cv.x, ex + s1, ex + s2};
    stl<u32x4>(l0 + L_HS + 16 * lane, st);
    tabv = u32x4{st.x | cv.x << 13, st.y | cv.y << 13, st.z | cv.z << 13, st.w | cv.w << 13};
  }
  {
    u32 sp[8];
#pragma unroll
    for (int j = 0; j < 8; ++j) sp[j] = ldl<u32>(l0 + L_HS + 4 * b[j]);
#pragma unroll
    for (int j = 0; j < 8; ++j) {
      const u32 eav = (j & 1) ? (a4[j >> 2][(j >> 1) & 1] & 0xffff0000u) : (a4[j >> 2][(j >> 1) & 1] << 16);
      const u32x2 rc = {s4[j >> 2][j & 3] | (d4[j >> 2][j & 3] - umul24(b[j], NPB)) << 16, eav};
      if (j < 4 ? act0 : act1) stl<u32x2>(l0 + L_SREC + 8 * (sp[j] + r[j]), rc);
    }
  }
  LBAR();
  STAMP(4);
  const __amdgpu_buffer_rsrc_t rrec = __builtin_amdgcn_make_buffer_rsrc((void*)(ws + W_REC), 0, NCH * CS * 8, 0x00020000);
  {
    const int n = min(CS, E - c * CS);
    u32x4 cp[4];
#pragma unroll
    for (int i = 0; i < 4; ++i) cp[i] = ldl<u32x4>(l0 + L_SREC + 16 * (tid + 512 * i));
#pragma unroll
    for (int i = 0; i < 4; ++i) { const int q = tid + 512 * i;
      __builtin_amdgcn_raw_buffer_store_b128(cp[i], rrec, 2 * q < n ? (c * CS + 2 * q) * 8 : 0x7ffffff0, 0, 16); }
  }
  STAMP(5);
  const u32 r4 = (lane >> 2) & 3, cofs = 32 * ((lane >> 4) & 1) + 8 * (lane & 3);
  const u32 wtr = (8 * hh + r4) * 128 + cofs;
  bf16x8 W[8];
  {
    const u32 wa0_ = l0 + L_WRAW + wtr + 64 * (r4 >> 1), wa1_ = l0 + L_WRAW + wtr + 64 - 64 * (r4 >> 1);
    if (w < 7) {
      f32x16 A0, A1;
#pragma unroll
      for (int q = 0; q < 16; ++q) { A0[q] = 0.f; A1[q] = 0.f; }
#pragma unroll
      for (int s = 0; s < 4; ++s) {
        A0 = MFMA(cat8(ldtr(wa0_ + s * 2048), ldtr(wa0_ + s * 2048 + 512)), hb[s], A0);
        A1 = MFMA(cat8(ldtr(wa1_ + s * 2048), ldtr(wa1_ + s * 2048 + 512)), hb[s], A1);
      }
      const u32 nl = 32 * w + e32;
      if (nl <= NPB) {
        const u32 a = l0 + L_HM + nl * RS + 8 * hh;
#pragma unroll
        for (int q = 0; q < 4; ++q) {
          stl<u32x2>(a + 16 * q, u32x2{cvtpk(A0[4 * q], A0[4 * q + 1]), cvtpk(A0[4 * q + 2], A0[4 * q + 3])});
          stl<u32x2>(a + 64 + 16 * q, u32x2{cvtpk(A1[4 * q], A1[4 * q + 1]), cvtpk(A1[4 * q + 2], A1[4 * q + 3])});
        }
      }
    }
#pragma unroll
    for (int i = 0; i < 8; ++i) { const u32 a = ((i & 1) ? wa1_ : wa0_) + (4 + (i >> 1)) * 2048; W[i] = cat8(ldtr(a), ldtr(a + 512)); }
    if (tid < NPB) { const u32 sh = (ptn & 1) << 4, xy = __builtin_amdgcn_alignbit(ptile.y, ptile.x, sh);
      stl<f32x4>(l0 + L_PT + 16 * tid, f32x4{lo2f(xy), hi2f(xy), __uint_as_float((ptile.y << (16 - sh)) & 0xffff0000u), 0.f}); }
  }
  STAMP(6);
  asm volatile("s_waitcnt vmcnt(0) lgkmcnt(0)\n\ts_barrier" ::: "memory");
  STAMP(7);
  const u32 tag = __builtin_amdgcn_readfirstlane((u32)epv) + 1u, tag6 = __builtin_amdgcn_readfirstlane((u32)(epv >> 32)) & 63u;
  if (w == 0) {
    const __amdgpu_buffer_rsrc_t rtab = __builtin_amdgcn_make_buffer_rsrc((void*)(ws + W_TAB), 0, NCH * NBK * 8, 0x00020000);
    const u32 t6 = tag6 << 26, o = (c * NBK + 4 * lane) * 8;
    __builtin_amdgcn_raw_buffer_store_b128(u32x4{tabv.x | t6, tag, tabv.y | t6, tag}, rtab, o, 0, 16);
    __builtin_amdgcn_raw_buffer_store_b128(u32x4{tabv.z | t6, tag, tabv.w | t6, tag}, rtab, o + 16, 0, 16);
  }
  if (n0 >= N) return;

  const int chunk0 = 32 * w + 8 * grp;
  u32 ent;
  {
    const char* tb = ws + W_TAB; const u32 vo = ((chunk0 + (j16 & 7)) * NBK + bk) * 8;
    u32x2 g; bool ok; u32 spins = 0;
    do {
      asm volatile("global_load_dwordx2 %0, %1, %2 sc1\n\ts_waitcnt vmcnt(0)" : "=&v"(g) : "v"(vo), "s"(tb) : "memory");
      ok = __builtin_amdgcn_ballot_w64(g.y != tag || (g.x >> 26) != tag6) == 0;
      if (!ok) __builtin_amdgcn_s_sleep(1);
    } while (!ok && ++spins < SPIN_MAX);
    ent = ok ? g.x : 0u;
    if (!ok && lane == 0) stl<u32>(l0 + L_MISC + 36, 1u);
  }
  STAMP(8);
  u32x2 rec[9]; u32 vmask = 0;
  u32 segb[8], segc[8];
  const u32 sb0 = (u32)chunk0 * (u32)CS + j16;
#define SEG_ENT(i) { const u32 e_ = dpp0<0x150 + i>(ent); segc[i] = (e_ >> 13) & 0x1fff; segb[i] = sb0 + (u32)(i * CS) + (e_ & 0x1fff); }
  SEG_ENT(0) SEG_ENT(1) SEG_ENT(2) SEG_ENT(3) SEG_ENT(4) SEG_ENT(5) SEG_ENT(6) SEG_ENT(7)
#define LDREC(idx) __builtin_bit_cast(u32x2, __builtin_amdgcn_raw_buffer_load_b64(rrec, (idx) * 8, 0, 16))
#pragma unroll
  for (int i = 0; i < 8; ++i) { const bool v = (u32)j16 < segc[i]; rec[i] = LDREC(v ? segb[i] : 0x0fffffffu); vmask |= (v ? 1u : 0u) << i; }
  u32 novf;
  {
    u32 ex[8], tot = 0, mx = 0;
#pragma unroll
    for (int i = 0; i < 8; ++i) { ex[i] = segc[i] > 16 ? segc[i] - 16 : 0; tot += ex[i]; mx = max(mx, ex[i]); }
    u32 inc = tot; inc += dpp0<0x142, 0xa>(inc); inc += dpp0<0x143, 0xc>(inc);
    novf = __builtin_amdgcn_readlane(inc, 63);
    u32 gb = inc - tot + j16;
    const u32 lst = l0 + L_OV + 256 * w;
    if (__builtin_amdgcn_ballot_w64(mx > 16) == 0) {
#pragma unroll
      for (int i = 0; i < 8; ++i) { stl<u32>(((u32)j16 < ex[i] && gb < 64) ? lst + 4 * gb : l0 + L_DUM + 4 * lane, segb[i] + 16); gb += ex[i]; }
    } else {
#pragma unroll
      for (int i = 0; i < 8; ++i) { for (u32 k = 0; k + j16 < ex[i]; k += 16) if (gb + k < 64) stl<u32>(lst + 4 * (gb + k), segb[i] + 16 + k); gb += ex[i]; }
    }
    const bool v = (u32)lane < novf; const u32 idx = ldl<u32>(lst + 4 * lane); rec[8] = LDREC(v ? idx : 0x0fffffffu); vmask |= (v ? 1u : 0u) << 8;
  }
  if (novf > 64 && lane == 0) stl<u32>(l0 + L_MISC + 36, 1u);
  STAMP(9);
  u32 rank[9]; u32x2 ps[9];
#pragma unroll
  for (int i = 0; i < 9; ++i) {
    ps[i] = __builtin_bit_cast(u32x2, __builtin_amdgcn_raw_buffer_load_b64(rpos, umul24(rec[i].x & 0xffff, 6u) & ~3u, 0, 0));
    rank[i] = __hip_atomic_fetch_add((LAS u32*)(size_t)((vmask >> i & 1) ? l0 + L_CNT + 4 * (rec[i].x >> 16) : l0 + L_DUM + 4 * lane), 1u, __ATOMIC_RELAXED, __HIP_MEMORY_SCOPE_WORKGROUP);
  }
  STAMP(10);
  LBAR();
  STAMP(11);
  if (blockIdx.x == 0 && tid == 0)
    __hip_atomic_store((gu64*)(ws + W_EP), (epv & 0xffffffff00000000ull) | tag, __ATOMIC_RELAXED, __HIP_MEMORY_SCOPE_AGENT);
  u32 wa0, wa1, wtb, wnt, wend; bool slow;
  {
    const u32x4 cv = ldl<u32x4>(l0 + L_CNT + 16 * lane);
    const u32 flag = ldl<u32>(l0 + L_MISC + 36);
    const u32 s1 = cv.x + cv.y, s2 = s1 + cv.z, s3 = s2 + cv.w;
    const u32 inc = wave_incl_scan(s3), ex = inc - s3;
    const u32 T = __builtin_amdgcn_readlane(inc, 63);
    const u32 off[4] = {ex, ex + cv.x, ex + s1, ex + s2};
    const float rT = T ? 8.f / (float)T : 0.f;
    u32 wv[4];
#pragma unroll
    for (int i = 0; i < 4; ++i) wv[i] = min(7u, (u32)((float)off[i] * rT));
    u32 prev = (u32)__builtin_amdgcn_update_dpp(-1, (int)wv[3], 0x138, 0xf, 0xf, false);
#pragma unroll
    for (int i = 0; i < 4; ++i) { const u32 n = 4 * lane + i;
      if (n < NPB && wv[i] != prev) stl<u32>(l0 + L_MISC + 4 * wv[i], off[i] << 8 | n);
      prev = wv[i]; }
    u32 cut = min(ldl<u32>(l0 + L_MISC + 4 * (lane & 7)), T << 8 | NPB);
    if (j16 >= 8) cut = T << 8 | NPB;
    cut = min(cut, (u32)__builtin_amdgcn_update_dpp(-1, (int)cut, 0x101, 0xf, 0xf, false));
    cut = min(cut, (u32)__builtin_amdgcn_update_dpp(-1, (int)cut, 0x102, 0xf, 0xf, false));
    cut = min(cut, (u32)__builtin_amdgcn_update_dpp(-1, (int)cut, 0x104, 0xf, 0xf, false));
    const u32 nxt = (u32)__builtin_amdgcn_update_dpp(-1, (int)cut, 0x101, 0xf, 0xf, false);
    u32 len = j16 < 8 ? (nxt >> 8) - (cut >> 8) : 0, nt = (len + 31) >> 5;
    u32 tb = nt; tb += dpp0<0x111>(tb); tb += dpp0<0x112>(tb); tb += dpp0<0x114>(tb);
    const u32 ttot = __builtin_amdgcn_readlane(tb, 7);
    tb -= nt;
    const u32 delta = 32 * tb - (cut >> 8);
    u32x4 op;
#pragma unroll
    for (int i = 0; i < 4; ++i) op[i] = off[i] + (u32)__builtin_amdgcn_ds_bpermute(4 * wv[i], (int)delta);
    stl<u32x4>(l0 + L_OFF + 16 * lane, op);
    wa0 = __builtin_amdgcn_readlane(cut, w) & 0xff; wa1 = __builtin_amdgcn_readlane(nxt, w) & 0xff;
    wtb = __builtin_amdgcn_readlane(tb, w); wnt = __builtin_amdgcn_readlane(nt, w);
    wend = 32 * wtb + __builtin_amdgcn_readlane(len, w);
    slow = ttot > MAXT || __builtin_amdgcn_readfirstlane(flag) != 0;
  }
  if (!slow) {
  STAMP(12);
  u32 pp[9];
#pragma unroll
  for (int i = 0; i < 9; ++i) pp[i] = ldl<u32>(l0 + L_OFF + 4 * (rec[i].x >> 16));
#pragma unroll
  for (int i = 0; i < 9; ++i) {
    const u32 p = (vmask >> i & 1) ? pp[i] + rank[i] : (u32)(MAXT * 32 + (lane & 31)); pp[i] = p;
    const u32 q = (p & ~31u) | (p & 7) << 2 | (p >> 3 & 3);
    stl<u16>(l0 + L_SR + 2 * q, (u16)rec[i].x); stl<unsigned char>(l0 + L_DS + q, (unsigned char)(rec[i].x >> 16));
  }
  {
    const u32 p = wend + lane;
    if (p < 32 * (wtb + wnt)) {
      const u32 q = (p & ~31u) | (p & 7) << 2 | (p >> 3 & 3);
      stl<u16>(l0 + L_SR + 2 * q, (u16)0); stl<unsigned char>(l0 + L_DS + q, (unsigned char)NPB);
      stl<u32x2>(l0 + L_V + 8 * p, u32x2{0, 0}); stl<u32>(l0 + L_DE + 4 * p, 0u);
    }
  }
  LBAR();
  STAMP(13);
  const Dma dh = {(const char*)h, (const char*)h - 1024, (const char*)h - 2048, (const char*)h - 3072};
  const u32 slot0 = l0 + L_STG + w * 8192;
  const u32 dch0 = 16 * ((lane & 7) ^ (lane >> 4)), dch1 = dch0 ^ 64;
  const u32 sraddr = l0 + L_SR + 64 * wtb + 8 * (lane >> 3);
  const u32 c128 = 128;
  auto issue = [&](u32 a, u32 b, u32x2 sv, u32 slot) {
    dma4_if(a, b, mad16lo(sv.x, c128, dch0), mad16hi(sv.x, c128, dch1), mad16lo(sv.y, c128, dch0), mad16hi(sv.y, c128, dch1), dh, slot);
  };
#pragma unroll
  for (int i = 0; i < 9; ++i) asm volatile("" : "+v"(ps[i].x), "+v"(ps[i].y));
  issue(0, wnt, ldl<u32x2>(sraddr), slot0);
  issue(1, wnt, ldl<u32x2>(sraddr + 64), slot0 + 4096);
  {
    f32x4 pd[9];
#pragma unroll
    for (int i = 0; i < 9; ++i) pd[i] = ldl<f32x4>(l0 + L_PT + 16 * (rec[i].x >> 16));
#pragma unroll
    for (int i = 0; i < 9; ++i) {
      const u32 sh = (rec[i].x & 1) << 4, xy = __builtin_amdgcn_alignbit(ps[i].y, ps[i].x, sh);
      const float dx = pd[i].x - lo2f(xy), dy = pd[i].y - hi2f(xy), dz = pd[i].z - __uint_as_float((ps[i].y << (16 - sh)) & 0xffff0000u);
      const float d2 = dx * dx + dy * dy + dz * dz;
      stl<u32x2>(l0 + L_V + 8 * pp[i], u32x2{cvtpk(1.f, dx), cvtpk(dy, dz)});
      stl<u32>(l0 + L_DE + 4 * pp[i], (cvtpk(d2, 0.f) & 0xffffu) | rec[i].y);
    }
  }
  STAMP(14);
  LBAR();
  STAMP(15);

  const u32 swz = (e32 >> 1) & 7;
  u32 sa[4];
#pragma unroll
  for (int s = 0; s < 4; ++s) sa[s] = slot0 + e32 * 128 + 16 * ((2 * s + hh) ^ swz);
  const u32 eq = 4 * hh + ((lane >> 2) & 3), slotA = 4 * ((lane >> 4) & 1) + (lane & 3);
  u32 zv = l0 + L_Z; asm volatile("" : "+v"(zv));
  u32 a_dsb = l0 + L_DS + 32 * wtb + 4 * (e32 & 7) + (e32 >> 3);
  u32 a_dsw = l0 + L_DS + 32 * wtb + 4 * eq;
  u32 a_de = l0 + L_DE + 128 * wtb + 4 * e32;
  u32 a_v = l0 + L_V + 256 * wtb + 8 * eq;
  u32 a_sr = sraddr;
  const u32 ohb = l0 + L_OH + (hh ? 144 : 0), ohl = hh ? 6 : 8, nh8 = (u32)(-8 * hh), dem = hh ? 0xffffffffu : 0u;
  const u32 pA = l0 + L_HM + (8 * hh + r4) * RS + cofs;
  const bool spc = hh && r4 >= 2;
  const u32 pB = spc ? l0 + L_WR + (r4 - 2) * 128 + cofs : pA + 4 * RS;
  u32 pM = spc ? 0 : 1; asm volatile("" : "+v"(pM));
  f32x16 Y0, Y1, X0, X1, ZERO;
#pragma unroll
  for (int r = 0; r < 16; ++r) { Y0[r] = 0.f; Y1[r] = 0.f; ZERO[r] = 0.f; }
  u32 base = wa0, tgt = wa0 + slotA;
  int bad = 0;
  const u32 fn = (lane & 31) >> 2, fi = lane & 3;
  u32 f_wx = l0 + L_WX + 16 * hh, f_m = l0 + L_HM + fn * RS + 8 * hh, f_p = l0 + L_PA + 16 * fn + 4 * (fi - 1);
  asm volatile("" : "+v"(f_wx), "+v"(f_m), "+v"(f_p));
  auto flush = [&]() {
    u32x4 c[4];
#pragma unroll
    for (int i = 0; i < 4; ++i) { c[0][i] = cvtpk(Y0[2 * i], Y0[2 * i + 1]); c[1][i] = cvtpk(Y0[8 + 2 * i], Y0[9 + 2 * i]);
                                  c[2][i] = cvtpk(Y1[2 * i], Y1[2 * i + 1]); c[3][i] = cvtpk(Y1[8 + 2 * i], Y1[9 + 2 * i]); }
    f32x16 D = ZERO;
#pragma unroll
    for (int i = 0; i < 4; ++i) D = MFMA(ldl<bf16x8>(f_wx + 32 * i), __builtin_bit_cast(bf16x8, c[i]), D);
    const u32 node = base + fn;
    if (node < wa1) {
      if (fi == 0) {
        const u32 a = f_m + base * RS;
#pragma unroll
        for (int q = 0; q < 4; ++q) {
          stl<u32x2>(a + 16 * q, u32x2{c[q >> 1][2 * (q & 1)], c[q >> 1][2 * (q & 1) + 1]});
          stl<u32x2>(a + 64 + 16 * q, u32x2{c[2 + (q >> 1)][2 * (q & 1)], c[2 + (q >> 1)][2 * (q & 1) + 1]});
        }
      } else if (hh == 0) stl<float>(f_p + 16 * base, D[0] * -LN2);
    }
  };
  if (wnt > 0) {
    bf16x8 As[4], Pb0, Pb1; u32x4 OH; u32x4 P0[2], P1[2];
    u32 lastc, lastn, aoh, aA, aB;
    u32 lastreal;
    { const u32 p = wend - 1, q = (p & ~31u) | (p & 7) << 2 | (p >> 3 & 3); lastreal = __builtin_amdgcn_readfirstlane((u32)ldl<unsigned char>(l0 + L_DS + q)); }
    float tt[4];
    auto front = [&](u32 dsb, u32 valid) {
      const u32 first = __builtin_amdgcn_readlane(dsb, 0); lastn = min((u32)__builtin_amdgcn_readlane(dsb, 31), lastreal);
      const u32 wb = min(first, (u32)(NPB - XW));
      bad = max(bad, (int)((lastn - wb) & valid));
      aoh = ohb + 16 * min(dsb - wb + nh8, ohl);
      const u32 wo = wb * RS;
      aA = pA + wo; aB = umul24(pM, wo) + pB;
    };
    auto rdwin = [&]() { OH = ldl<u32x4>(aoh);
      Pb0 = cat8(ldtr(aA), ldtr(aB)); Pb1 = cat8(ldtr(aA + 64), ldtr(aB + 64)); };
#define EA(X, b) { _Pragma("unroll") for (int k_ = 0; k_ < 4; ++k_) tt[k_] = __builtin_amdgcn_exp2f(X[b + k_]); SB(); \
                   _Pragma("unroll") for (int k_ = 0; k_ < 4; ++k_) tt[k_] = 1.f + tt[k_]; SB(); }
#define RMC(X, b, P) { _Pragma("unroll") for (int k_ = 0; k_ < 4; ++k_) tt[k_] = __builtin_amdgcn_rcpf(tt[k_]); SB(); \
                   _Pragma("unroll") for (int k_ = 0; k_ < 4; ++k_) tt[k_] = vmul(tt[k_], X[b + k_]); SB(); \
                   P[(b) >> 3][((b) >> 1) & 3] = cvtpk(tt[0], tt[1]); P[(b) >> 3][(((b) >> 1) & 3) + 1] = cvtpk(tt[2], tt[3]); SB(); }
#define BF(x) __builtin_bit_cast(bf16x8, x)
    {
      const u32 dsb = ldl<unsigned char>(a_dsb), de = ldl<u32>(a_de); const u32x2 sv = ldl<u32x2>(a_sr + 128);
      wait_dma(1, wnt);
#pragma unroll
      for (int s = 0; s < 4; ++s) As[s] = ldl<bf16x8>(sa[s]);
      front(dsb, ~0u); lastc = lastn;
      rdwin();
      asm volatile("s_waitcnt lgkmcnt(0)" ::: "memory");
      issue(2, wnt, sv, slot0);
      OH.w |= de & dem;
      X0 = MFMA(As[0], W[0], ZERO); X0 = MFMA(As[1], W[2], X0); X0 = MFMA(As[2], W[4], X0); X0 = MFMA(As[3], W[6], X0); X0 = MFMA(BF(OH), Pb0, X0);
    }
    auto body = [&](auto uc, u32 t) {
      constexpr int U = decltype(uc)::value;
      const u32 dsw = ldl<u32>(a_dsw + 32 * U), dsb = ldl<unsigned char>(a_dsb + 32 * (U + 1)), de = ldl<u32>(a_de + 128 * (U + 1));
      const u32x2 sv = ldl<u32x2>(a_sr + 64 * (U + 3));
      SB();
      X1 = MFMA(BF(OH), Pb1, ZERO); SB();
      EA(X0, 0)
      X1 = MFMA(As[0], W[1], X1); SB();
      RMC(X0, 0, P0)
      X1 = MFMA(As[1], W[3], X1); SB();
      EA(X0, 4)
      X1 = MFMA(As[2], W[5], X1); SB();
      RMC(X0, 4, P0)
      X1 = MFMA(As[3], W[7], X1); SB();
      wait_dma(t + 2, wnt);
#pragma unroll
      for (int s = 0; s < 4; ++s) As[s] = ldl<bf16x8>(sa[s] + 4096 * (U ^ 1));
      SB();
      front(dsb, t + 1 < wnt ? ~0u : 0u);
      u32 sel[4];
#pragma unroll
      for (int a = 0; a < 4; ++a) sel[a] = ((dsw >> (8 * a)) & 0xff) == tgt ? a_v : zv;
      SB();
      EA(X0, 8) RMC(X0, 8, P0)
      asm volatile("s_waitcnt lgkmcnt(0)" ::: "memory");
      issue(t + 3, wnt, sv, slot0 + 4096 * (U ^ 1));
      rdwin();
      s16x4 Bq[4];
#pragma unroll
      for (int a = 0; a < 4; ++a) Bq[a] = ldtr(sel[a] + 64 * a + 256 * U);
      SB();
      EA(X0, 12) RMC(X0, 12, P0)
      X0 = MFMA(As[0], W[0], ZERO); SB();
      EA(X1, 0)
      X0 = MFMA(As[1], W[2], X0); SB();
      RMC(X1, 0, P1)
      X0 = MFMA(As[2], W[4], X0); SB();
      EA(X1, 4)
      X0 = MFMA(As[3], W[6], X0); SB();
      RMC(X1, 4, P1)
      OH.w |= de & dem; SB();
      X0 = MFMA(BF(OH), Pb0, X0); SB();
      EA(X1, 8)
      bf16x8 B0 = cat8(Bq[0], Bq[1]), B1 = cat8(Bq[2], Bq[3]);
      Y0 = MFMA(BF(P0[0]), B0, Y0); SB();
      RMC(X1, 8, P1)
      Y0 = MFMA(BF(P0[1]), B1, Y0); SB();
      EA(X1, 12)
      Y1 = MFMA(BF(P1[0]), B0, Y1); SB();
      RMC(X1, 12, P1)
      Y1 = MFMA(BF(P1[1]), B1, Y1); SB();
      if (lastc >= base + 8) do {
        flush(); base += 8; tgt += 8;
        asm volatile("" : "+s"(base), "+v"(tgt));
#pragma unroll
        for (int a = 0; a < 4; ++a) Bq[a] = ldtr((((dsw >> (8 * a)) & 0xff) == tgt ? a_v : zv) + 64 * a + 256 * U);
        B0 = cat8(Bq[0], Bq[1]); B1 = cat8(Bq[2], Bq[3]);
        Y0 = MFMA(BF(P0[0]), B0, ZERO); Y1 = MFMA(BF(P1[0]), B0, ZERO);
        Y0 = MFMA(BF(P0[1]), B1, Y0); Y1 = MFMA(BF(P1[1]), B1, Y1);
      } while (lastc >= base + 8);
      lastc = lastn;
    };
    for (u32 t = 0;; t += 2) {
      body(IC<0>{}, t);
      if (t + 1 >= wnt) break;
      body(IC<1>{}, t + 1);
      if (t + 2 >= wnt) break;
      a_dsb += 64; a_dsw += 64; a_de += 256; a_v += 512; a_sr += 128;
    }
  }
  STAMP(16);
  {
    const u32 rb = (u32)(n0 + 32 * w) + (lane >> 3), nm = (u32)N - 1;
    dma4(min(rb, nm) * 128 + dch0, min(rb + 8, nm) * 128 + dch1, min(rb + 16, nm) * 128 + dch0, min(rb + 24, nm) * 128 + dch1, dh, slot0);
    u32 sv; const char* g = (const char*)wh + w * 2048; const u32 vo = (lane * 16) ^ ((lane >> 4 & 1) << 6);
    asm volatile("s_mov_b32 %0, m0\n\ts_mov_b32 m0, %3\n\ts_nop 0\n\tglobal_load_lds_dwordx4 %1, %2\n\tglobal_load_lds_dwordx4 %1, %2 offset:1024\n\ts_mov_b32 m0, %0"
                 : "=&s"(sv) : "v"(vo), "s"(g), "s"(slot0 + 4096) : "memory");
  }
  if (base < wa1) { flush(); base += 8; }
  if (base < wa1) {
#pragma unroll
    for (int r = 0; r < 16; ++r) { Y0[r] = 0.f; Y1[r] = 0.f; }
    do { flush(); base += 8; } while (base < wa1);
  }
  if (bad >= XW && lane == 0) stl<u32>(l0 + L_MISC + 36, 1u);
  STAMP(17);
  asm volatile("s_waitcnt vmcnt(0) lgkmcnt(0)\n\ts_barrier" ::: "memory");
  STAMP(18);
  slow = __builtin_amdgcn_readfirstlane(ldl<u32>(l0 + L_MISC + 36)) != 0;

  if (!slow) {
  if (w < 7) {
    const u32 nl = 32 * w + e32, mrow = l0 + L_HM + min(nl, (u32)NPB) * RS + 16 * hh;
    f32x16 A0, A1, C0, C1;
#pragma unroll
    for (int r = 0; r < 16; ++r) { A0[r] = 0.f; A1[r] = 0.f; C0[r] = 0.f; C1[r] = 0.f; }
#pragma unroll
    for (int s = 0; s < 4; ++s) {
      const bf16x8 hv = ldl<bf16x8>(sa[s]), mb = ldl<bf16x8>(mrow + 32 * s);
      const u32 wa = l0 + L_STG + 4096 + wtr + 64 * (r4 >> 1), wb = l0 + L_STG + 4096 + wtr + 64 - 64 * (r4 >> 1);
      const bf16x8 w0 = cat8(ldtr(wa + s * 8192), ldtr(wa + s * 8192 + 512)), w1 = cat8(ldtr(wb + s * 8192), ldtr(wb + s * 8192 + 512));
      const bf16x8 w2 = cat8(ldtr(wa + (4 + s) * 8192), ldtr(wa + (4 + s) * 8192 + 512)), w3 = cat8(ldtr(wb + (4 + s) * 8192), ldtr(wb + (4 + s) * 8192 + 512));
      A0 = MFMA(w0, hv, A0); A1 = MFMA(w1, hv, A1); C0 = MFMA(w2, mb, C0); C1 = MFMA(w3, mb, C1);
    }
#pragma unroll
    for (int ft = 0; ft < 2; ++ft)
#pragma unroll
      for (int q = 0; q < 4; ++q) {
        const u32 a = slot0 + e32 * 128 + 16 * ((4 * ft + q) ^ swz) + 8 * hh;
        const u32x2 hv = ldl<u32x2>(a);
        float o[4];
#pragma unroll
        for (int k = 0; k < 4; ++k) { const float z = ft ? A1[4 * q + k] - LN2 * C1[4 * q + k] : A0[4 * q + k] - LN2 * C0[4 * q + k]; o[k] = silu_f(z); }
        stl<u32x2>(a, u32x2{cvtpk(lo2f(hv.x) + o[0], hi2f(hv.x) + o[1]), cvtpk(lo2f(hv.y) + o[2], hi2f(hv.y) + o[3])});
      }
    asm volatile("s_waitcnt lgkmcnt(0)" ::: "memory");
#pragma unroll
    for (int a = 0; a < 4; ++a) {
      const u32 row = 8 * a + (lane >> 3), nr = 32 * w + row;
      const u32x4 v = ldl<u32x4>(slot0 + a * 1024 + lane * 16);
      if (nr < NPB && n0 + nr < (u32)N) *(u32x4*)((char*)out + (size_t)(n0 + nr) * 128 + ((a & 1) ? dch1 : dch0)) = v;
    }
  } else {
    const int nn = min(NPB, N - n0);
    u32* po = (u32*)(out + (size_t)N * 64 + (size_t)n0 * 3);
    for (int i = lane; 2 * i < 3 * nn; i += 64) {
      const int e0 = 2 * i, e1 = e0 + 1, a0 = e0 / 3, a1 = e1 / 3, c0 = e0 - 3 * a0, c1 = e1 - 3 * a1;
      const float v0 = ldl<float>(l0 + L_PT + 16 * a0 + 4 * c0) + ldl<float>(l0 + L_PA + 16 * a0 + 4 * c0);
      const float v1 = ldl<float>(l0 + L_PT + 16 * a1 + 4 * c1) + ldl<float>(l0 + L_PA + 16 * a1 + 4 * c1);
      po[i] = cvtpk(v0, v1);
    }
  }
  STAMP(19);
  }
  }
  if (slow) slow_bucket(h, pos, ea, src, dst, we, wx, wh, out, N, E, bk, l0);
  STAMP_DUMP(160u << 20, bk * 8 + w);
}

__global__ void gen_zero(float* p, int n) { int i = blockIdx.x * blockDim.x + threadIdx.x; if (i < n) p[i] = 0.f; }
__global__ void gen_edge(const u16* h, const u16* pos, const u16* ea, const int* src, const int* dst, const u16* we, const u16* wx, float* mi, float* pd, int E) {
  const int lane = threadIdx.x & 63, w = threadIdx.x >> 6, nw = blockDim.x >> 6;
  const float wxf = bf2f(wx[lane]);
  for (int e = blockIdx.x * nw + w; e < E; e += gridDim.x * nw) {
    const int s = src[e], d = dst[e];
    float df[3], d2 = 0.f;
#pragma unroll
    for (int k = 0; k < 3; ++k) { df[k] = lo2f(cvtpk(bf2f(pos[(size_t)d * 3 + k]) - bf2f(pos[(size_t)s * 3 + k]), 0.f)); d2 += df[k] * df[k]; }
    d2 = lo2f(cvtpk(d2, 0.f));
    const float hd = bf2f(h[(size_t)d * 64 + lane]), hs = bf2f(h[(size_t)s * 64 + lane]);
    float z = 0.f;
#pragma unroll 4
    for (int k = 0; k < 64; ++k) z += __shfl(hd, k) * bf2f(we[k * 64 + lane]) + __shfl(hs, k) * bf2f(we[(64 + k) * 64 + lane]);
    z += d2 * bf2f(we[128 * 64 + lane]) + bf2f(ea[e]) * bf2f(we[129 * 64 + lane]);
    const float m = lo2f(cvtpk(silu_f(z), 0.f));
    float cs = m * wxf;
#pragma unroll
    for (int o = 32; o > 0; o >>= 1) cs += __shfl_xor(cs, o);
    atomicAdd(&mi[(size_t)d * 64 + lane], m);
    if (lane < 3) atomicAdd(&pd[(size_t)d * 3 + lane], (lane == 0 ? df[0] : lane == 1 ? df[1] : df[2]) * cs);
  }
}
__global__ void gen_node(const u16* h, const u16* pos, const u16* wh, const float* mi, const float* pd, u16* out, int N) {
  const int lane = threadIdx.x & 63, w = threadIdx.x >> 6, nw = blockDim.x >> 6;
  for (int n = blockIdx.x * nw + w; n < N; n += gridDim.x * nw) {
    const float hv = bf2f(h[(size_t)n * 64 + lane]), mv = lo2f(cvtpk(mi[(size_t)n * 64 + lane], 0.f));
    float z = 0.f;
#pragma unroll 4
    for (int k = 0; k < 64; ++k) z += __shfl(hv, k) * bf2f(wh[k * 64 + lane]) + __shfl(mv, k) * bf2f(wh[(64 + k) * 64 + lane]);
    out[(size_t)n * 64 + lane] = (u16)cvtpk(hv + silu_f(z), 0.f);
    if (lane < 3) out[(size_t)N * 64 + (size_t)n * 3 + lane] = (u16)cvtpk(bf2f(pos[(size_t)n * 3 + lane]) + pd[(size_t)n * 3 + lane], 0.f);
  }
}

extern "C" void kernel_launch(void* const* d_in, const int* in_sizes, int n_in, void* d_out, int out_size, void* d_ws, size_t ws_size, hipStream_t stream) {
  const u16* h = (const u16*)d_in[0];
  const u16* pos = (const u16*)d_in[1];
  const u16* ea = (const u16*)d_in[2];
  const int* src = (const int*)d_in[3];
  const int* dst = (const int*)d_in[4];
  const u16* we = (const u16*)d_in[5];
  const u16* wx = (const u16*)d_in[6];
  const u16* wh = (const u16*)d_in[7];
  const int N = in_sizes[0] / 64, E = in_sizes[3];
  const int CS = ((E + NCH - 1) / NCH + 3) & ~3;
  const bool fast = N >= 2 && N <= NPB * NBK && (N & 1) == 0 && (E & 3) == 0 && E > 0 && CS <= 4096 && W_REC + (size_t)NCH * CS * 8 <= ws_size;
  if (fast) {
    (void)hipFuncSetAttribute((const void*)egnn_fused, hipFuncAttributeMaxDynamicSharedMemorySize, L_END);
    egnn_fused<<<NBK, 512, L_END, stream>>>(h, pos, ea, src, dst, we, wx, wh, (char*)d_ws, (u16*)d_out, N, E, CS);
  } else {
    float* mi = (float*)d_ws; float* pd = mi + (size_t)N * 64;
    const int nz = N * 67;
    gen_zero<<<(nz + 255) / 256, 256, 0, stream>>>(mi, nz);
    gen_edge<<<2048, 256, 0, stream>>>(h, pos, ea, src, dst, we, wx, mi, pd, E);
    gen_node<<<1024, 256, 0, stream>>>(h, pos, wh, mi, pd, (u16*)d_out, N);
  }
}
```

```cpp
#include <hip/hip_runtime.h>
#include <hip/hip_bf16.h>
#include <stdint.h>

typedef unsigned short u16;
typedef unsigned u32;
typedef short bf16x8 __attribute__((ext_vector_type(8)));
typedef short s16x4 __attribute__((ext_vector_type(4)));
typedef float f32x16 __attribute__((ext_vector_type(16)));
typedef float f32x4 __attribute__((ext_vector_type(4)));
typedef unsigned u32x4 __attribute__((ext_vector_type(4)));
typedef unsigned u32x2 __attribute__((ext_vector_type(2)));
#define LAS __attribute__((address_space(3)))

constexpr int NPB = 196;
constexpr int NBK = 256;
constexpr int NCH = 256;
constexpr int MAXT = 112;
constexpr float LOG2E = 1.4426950408889634f, LN2 = 0.6931471805599453f;

constexpr int NT1 = MAXT + 1;
constexpr int RS = 144;
constexpr int XW = 14;
constexpr int L_STG = 0;
constexpr int L_R = 65536;
constexpr int L_HM = L_R + NT1 * 512;
constexpr int L_PT = L_HM + 197 * RS;
constexpr int L_PA = L_PT + NPB * 16;
constexpr int L_CNT = L_PA + NPB * 16;
constexpr int L_WX = L_CNT + 1024;
constexpr int L_WR = L_WX + 128;
constexpr int L_OH = L_WR + 256;
constexpr int L_Z = L_OH + 256;
constexpr int L_MISC = L_Z + 1024;
constexpr int L_END = L_MISC + 64;
constexpr int L_SREC = L_STG;
constexpr int L_WRAW = L_STG + 32768;
constexpr int L_HC = L_STG + 49152;
constexpr int L_HS = L_HC + 1024;
static_assert(L_HM % 16 == 0 && L_PT % 16 == 0 && L_CNT % 16 == 0 && L_Z % 16 == 0 && L_WX % 16 == 0 && L_OH % 16 == 0, "align");
static_assert(L_END <= 160 * 1024, "lds");

constexpr size_t W_EP = 0;
constexpr size_t W_TAB = 4096;
constexpr size_t W_REC = W_TAB + NCH * NBK * 8;

static __device__ __forceinline__ float bf2f(u32 v) { return __uint_as_float(v << 16); }
typedef __bf16 bf16x2_t __attribute__((ext_vector_type(2)));
typedef float f32x2 __attribute__((ext_vector_type(2)));
static __device__ __forceinline__ u32 cvtpk(float lo, float hi) { return __builtin_bit_cast(u32, __builtin_convertvector(f32x2{lo, hi}, bf16x2_t)); }
static __device__ __forceinline__ float lo2f(u32 v) { return __uint_as_float(v << 16); }
static __device__ __forceinline__ float hi2f(u32 v) { return __uint_as_float(v & 0xffff0000u); }
template <int CTRL, int RM = 0xf> static __device__ __forceinline__ u32 dpp0(u32 x) { return (u32)__builtin_amdgcn_update_dpp(0, (int)x, CTRL, RM, 0xf, true); }
static __device__ __forceinline__ u32 wave_incl_scan(u32 x) {
  x += dpp0<0x111>(x); x += dpp0<0x112>(x); x += dpp0<0x114>(x); x += dpp0<0x118>(x);
  x += dpp0<0x142, 0xa>(x); x += dpp0<0x143, 0xc>(x); return x; }
#define MFMA(a, b, c) __builtin_amdgcn_mfma_f32_32x32x16_bf16(a, b, c, 0, 0, 0)
#define SB() __builtin_amdgcn_sched_barrier(0)
#define LBAR() asm volatile("s_waitcnt lgkmcnt(0)\n\ts_barrier" ::: "memory")
template <class T> static __device__ __forceinline__ T ldl(u32 a) { return *(LAS const T*)(size_t)a; }
template <class T> static __device__ __forceinline__ void stl(u32 a, T v) { *(LAS T*)(size_t)a = v; }
static __device__ __forceinline__ s16x4 ldtr(u32 a) { typedef short v4 __attribute__((ext_vector_type(4)));
  return __builtin_bit_cast(s16x4, __builtin_amdgcn_ds_read_tr16_b64_v4i16((LAS v4*)(size_t)a)); }
static __device__ __forceinline__ float silu_f(float z) { return z * __builtin_amdgcn_rcpf(1.f + __builtin_amdgcn_exp2f(-LOG2E * z)); }

#define STAMP(i)
#define STAMP_DECL
#define STAMP_DUMP(base, idx)
struct Dma { const char* b0; const char* b1; const char* b2; const char* b3; };
static __device__ __forceinline__ void dma4(u32 v0, u32 v1, u32 v2, u32 v3, const Dma& d, u32 lds) {
  u32 sv;
  asm volatile("s_mov_b32 %0, m0\n\ts_mov_b32 m0, %9\n\ts_nop 0\n\t"
               "global_load_lds_dwordx4 %1, %5\n\t"
               "global_load_lds_dwordx4 %2, %6 offset:1024\n\t"
               "global_load_lds_dwordx4 %3, %7 offset:2048\n\t"
               "global_load_lds_dwordx4 %4, %8 offset:3072\n\t"
               "s_mov_b32 m0, %0"
               : "=&s"(sv) : "v"(v0), "v"(v1), "v"(v2), "v"(v3), "s"(d.b0), "s"(d.b1), "s"(d.b2), "s"(d.b3), "s"(lds) : "memory");
}

static __device__ __forceinline__ void slow_bucket(const u16* __restrict__ h, const u16* __restrict__ pos, const u16* __restrict__ ea, const int* __restrict__ src,
                                                const int* __restrict__ dst, const u16* __restrict__ we, const u16* __restrict__ wx,
                                                const u16* __restrict__ wh, u16* __restrict__ out, int N, int E, int bk, u32 l0) {
  const int tid = threadIdx.x, lane = tid & 63, w = tid >> 6, n0 = bk * NPB;
  LAS float* macc = (LAS float*)(size_t)(l0 + L_STG);
  LAS float* pacc = (LAS float*)(size_t)(l0 + L_PA);
  __syncthreads();
  for (int i = tid; i < NPB * 64; i += 512) macc[i] = 0.f;
  for (int i = tid; i < NPB * 4; i += 512) pacc[i] = 0.f;
  __syncthreads();
  const float wxf = bf2f(wx[lane]);
  for (int eb = 64 * w; eb < E; eb += 512) {
    const int dv = eb + lane < E ? dst[eb + lane] : -1;
    unsigned long long m = __builtin_amdgcn_ballot_w64((u32)(dv - n0) < (u32)NPB && dv >= 0);
    while (m) {
      const int l = __builtin_ctzll(m); m &= m - 1;
      const int e = eb + l, d = __builtin_amdgcn_readlane(dv, l), s = src[e], dl = d - n0;
      float df[3], d2 = 0.f;
#pragma unroll
      for (int k = 0; k < 3; ++k) { df[k] = lo2f(cvtpk(bf2f(pos[d * 3 + k]) - bf2f(pos[s * 3 + k]), 0.f)); d2 += df[k] * df[k]; }
      d2 = lo2f(cvtpk(d2, 0.f));
      const float hd = bf2f(h[(size_t)d * 64 + lane]), hs = bf2f(h[(size_t)s * 64 + lane]);
      float z = 0.f;
#pragma unroll 4
      for (int k = 0; k < 64; ++k) z += __shfl(hd, k) * bf2f(we[k * 64 + lane]) + __shfl(hs, k) * bf2f(we[(64 + k) * 64 + lane]);
      z += d2 * bf2f(we[128 * 64 + lane]) + bf2f(ea[e]) * bf2f(we[129 * 64 + lane]);
      const float mm = lo2f(cvtpk(silu_f(z), 0.f));
      float cs = mm * wxf;
#pragma unroll
      for (int o = 32; o > 0; o >>= 1) cs += __shfl_xor(cs, o);
      atomicAdd((float*)&macc[dl * 64 + lane], mm);
      if (lane < 3) atomicAdd((float*)&pacc[dl * 4 + lane], (lane == 0 ? df[0] : lane == 1 ? df[1] : df[2]) * cs);
    }
  }
  __syncthreads();
  for (int nl = w; nl < NPB; nl += 8) {
    const int n = n0 + nl; if (n >= N) break;
    const float hv = bf2f(h[(size_t)n * 64 + lane]), mv = lo2f(cvtpk(macc[nl * 64 + lane], 0.f));
    float z = 0.f;
#pragma unroll 4
    for (int k = 0; k < 64; ++k) z += __shfl(hv, k) * bf2f(wh[k * 64 + lane]) + __shfl(mv, k) * bf2f(wh[(64 + k) * 64 + lane]);
    out[(size_t)n * 64 + lane] = (u16)cvtpk(hv + silu_f(z), 0.f);
    if (lane < 3) out[(size_t)N * 64 + (size_t)n * 3 + lane] = (u16)cvtpk(bf2f(pos[n * 3 + lane]) + pacc[nl * 4 + lane], 0.f);
  }
}
static __device__ __forceinline__ u32 umul24(u32 a, u32 b) { return (u32)__umul24(a, b); }
static __device__ __forceinline__ u32 mad16lo(u32 a, u32 b, u32 c) { u32 r; asm("v_mad_u32_u16 %0, %1, %2, %3" : "=v"(r) : "v"(a), "s"(b), "v"(c)); return r; }
static __device__ __forceinline__ u32 mad16hi(u32 a, u32 b, u32 c) { u32 r; asm("v_mad_u32_u16 %0, %1, %2, %3 op_sel:[1,0,0,0]" : "=v"(r) : "v"(a), "s"(b), "v"(c)); return r; }
static __device__ __forceinline__ bf16x8 cat8(s16x4 a, s16x4 b) { return __builtin_shufflevector(a, b, 0, 1, 2, 3, 4, 5, 6, 7); }
extern "C" __device__ float llvm_fmul_legacy(float, float) __asm("llvm.amdgcn.fmul.legacy");
static __device__ __forceinline__ float vmul(float a, float b) { return llvm_fmul_legacy(a, b); }
static __device__ __forceinline__ void wait_dma(u32 a, u32 b) {
  asm volatile("s_cmp_lt_u32 %0, %1\n\ts_cbranch_scc1 1f\n\ts_waitcnt vmcnt(0)\n1:\n\ts_waitcnt vmcnt(4)" :: "s"(a), "s"(b) : "scc", "memory"); }
static __device__ __forceinline__ void dma4_if(u32 a, u32 b, u32 v0, u32 v1, u32 v2, u32 v3, const Dma& d, u32 lds) {
  u32 sv;
  asm volatile("s_cmp_lt_u32 %10, %11\n\ts_cbranch_scc0 1f\n\t"
               "s_mov_b32 %0, m0\n\ts_mov_b32 m0, %9\n\ts_nop 0\n\t"
               "global_load_lds_dwordx4 %1, %5\n\t"
               "global_load_lds_dwordx4 %2, %6 offset:1024\n\t"
               "global_load_lds_dwordx4 %3, %7 offset:2048\n\t"
               "global_load_lds_dwordx4 %4, %8 offset:3072\n\t"
               "s_mov_b32 m0, %0\n1:"
               : "=&s"(sv) : "v"(v0), "v"(v1), "v"(v2), "v"(v3), "s"(d.b0), "s"(d.b1), "s"(d.b2), "s"(d.b3), "s"(lds), "s"(a), "s"(b) : "scc", "memory");
}
template <int V> struct IC { static constexpr int value = V; };

constexpr u32 SPIN_MAX = 1u << 15;
typedef __attribute__((address_space(1))) unsigned long long gu64;

__global__ void __launch_bounds__(512, 2) egnn_fused(const u16* __restrict__ h, const u16* __restrict__ pos, const u16* __restrict__ ea, const int* __restrict__ src,
                                                     const int* __restrict__ dst, const u16* __restrict__ we, const u16* __restrict__ wx, const u16* __restrict__ wh,
                                                     char* __restrict__ ws, u16* __restrict__ out, int N, int E, int CS) {
  extern __shared__ __attribute__((aligned(16))) char smem[];
  const u32 l0 = (u32)(size_t)(LAS char*)smem;
  const int tid = threadIdx.x, lane = tid & 63;
  const int w = __builtin_amdgcn_readfirstlane(tid >> 6);
  const int c = blockIdx.x;
  const int bk = (blockIdx.x & 7) * 32 + (blockIdx.x >> 3);
  const int n0 = bk * NPB;
  STAMP_DECL STAMP(0);

  const int grp = lane >> 4, j16 = lane & 15, e32 = lane & 31, hh = lane >> 5;
  const int e0 = c * CS + 4 * tid;
  const bool act0 = 4 * tid < CS && e0 < E, act1 = 4 * tid + 2048 < CS && e0 + 2048 < E;
  const __amdgpu_buffer_rsrc_t rdst = __builtin_amdgcn_make_buffer_rsrc((void*)dst, 0, E * 4, 0x00020000), rsrc_ = __builtin_amdgcn_make_buffer_rsrc((void*)src, 0, E * 4, 0x00020000);
  const __amdgpu_buffer_rsrc_t rea = __builtin_amdgcn_make_buffer_rsrc((void*)ea, 0, E * 2, 0x00020000);
  const u32 eo0 = act0 ? (u32)e0 : 0x1fffff00u, eo1 = act1 ? (u32)e0 + 2048 : 0x1fffff00u;
  u32x4 d4[2], s4[2]; u32x2 a4[2];
  d4[0] = __builtin_bit_cast(u32x4, __builtin_amdgcn_raw_buffer_load_b128(rdst, eo0 * 4, 0, 0));
  d4[1] = __builtin_bit_cast(u32x4, __builtin_amdgcn_raw_buffer_load_b128(rdst, eo1 * 4, 0, 0));
  s4[0] = __builtin_bit_cast(u32x4, __builtin_amdgcn_raw_buffer_load_b128(rsrc_, eo0 * 4, 0, 0));
  s4[1] = __builtin_bit_cast(u32x4, __builtin_amdgcn_raw_buffer_load_b128(rsrc_, eo1 * 4, 0, 0));
  a4[0] = __builtin_bit_cast(u32x2, __builtin_amdgcn_raw_buffer_load_b64(rea, eo0 * 2, 0, 0));
  a4[1] = __builtin_bit_cast(u32x2, __builtin_amdgcn_raw_buffer_load_b64(rea, eo1 * 2, 0, 0));
  const unsigned long long epv = __hip_atomic_load((gu64*)(ws + W_EP), __ATOMIC_RELAXED, __HIP_MEMORY_SCOPE_AGENT);
  u32x4 wt[2];
#pragma unroll
  for (int i = 0; i < 2; ++i) wt[i] = ((const u32x4*)we)[tid + 512 * i];
  const u32 wxr = wx[16 * ((tid >> 4) & 3) + (tid & 3) + 8 * ((tid >> 2) & 1) + 4 * ((tid >> 3) & 1)];
  const u32 wrr = we[128 * 64 + (tid & 127)];
  if (tid < 256) { stl<u32>(l0 + L_CNT + 4 * tid, 0u); stl<u32>(l0 + L_HC + 4 * tid, 0u); }
  if (tid < 16) stl<u32>(l0 + L_MISC + 4 * tid, tid < 8 ? 0xffffffffu : 0u);
  if (tid < 256) stl<u32>(l0 + L_Z + 4 * tid, 0u);
  const u32 lst = l0 + L_R + 2048 * w;
  stl<u32x4>(lst + 32 * lane, u32x4{8, 8, 8, 8}); stl<u32x4>(lst + 32 * lane + 16, u32x4{8, 8, 8, 8});
  if (tid < NPB) stl<f32x4>(l0 + L_PA + 16 * tid, f32x4{0, 0, 0, 0});
  if (tid < 64) { const int u = tid < 36 ? tid : tid - 36, e = u >> 2, lim = tid < 36 ? 8 : 6;
    stl<u32>(l0 + L_OH + 4 * tid, (e < lim && (e >> 1) == (u & 3)) ? ((e & 1) ? 0x3f800000u : 0x3f80u) : 0u); }
  LBAR();
  STAMP(1);
  asm volatile("" :: "v"(d4[0]), "v"(d4[1]) : "memory");
  STAMP(2);
  const int nmax = N - 1;
  const __amdgpu_buffer_rsrc_t rpos = __builtin_amdgcn_make_buffer_rsrc((void*)pos, 0, N * 6 + 2, 0x00020000);
  bf16x8 hb[4];
  { const u16* hr = h + (size_t)min(n0 + 32 * w + e32, nmax) * 64 + 8 * hh;
#pragma unroll
    for (int s = 0; s < 4; ++s) hb[s] = *(const bf16x8*)(hr + 16 * s); }
  const u32 ptn = min(n0 + tid, nmax);
  const u32x2 ptile = __builtin_bit_cast(u32x2, __builtin_amdgcn_raw_buffer_load_b64(rpos, ptn * 6, 0, 0));
  u32 b[8], r[8];
#pragma unroll
  for (int j = 0; j < 8; ++j) b[j] = umul24(d4[j >> 2][j & 3], 85599u) >> 24;
  if (act0) {
#pragma unroll
    for (int j = 0; j < 4; ++j) r[j] = __hip_atomic_fetch_add((LAS u32*)(size_t)(l0 + L_HC + 4 * b[j]), 1u, __ATOMIC_RELAXED, __HIP_MEMORY_SCOPE_WORKGROUP);
  }
  if (act1) {
#pragma unroll
    for (int j = 4; j < 8; ++j) r[j] = __hip_atomic_fetch_add((LAS u32*)(size_t)(l0 + L_HC + 4 * b[j]), 1u, __ATOMIC_RELAXED, __HIP_MEMORY_SCOPE_WORKGROUP);
  }
#pragma unroll
  for (int i = 0; i < 2; ++i) {
    const u32 q = tid + 512 * i;
    u32x4 o;
#pragma unroll
    for (int k = 0; k < 4; ++k) o[k] = cvtpk(lo2f(wt[i][k]) * -LOG2E, hi2f(wt[i][k]) * -LOG2E);
    stl<u32x4>(l0 + L_WRAW + ((q * 16) ^ ((q >> 4 & 1) << 6)), o);
  }
  if (tid < 64) stl<u16>(l0 + L_WX + 2 * tid, (u16)wxr);
  if (tid < 128) stl<u16>(l0 + L_WR + 2 * tid, (u16)cvtpk(bf2f(wrr) * -LOG2E, 0.f));
  LBAR();
  STAMP(3);
  const u32 tag = __builtin_amdgcn_readfirstlane((u32)epv) + 1u, tag6 = __builtin_amdgcn_readfirstlane((u32)(epv >> 32)) & 63u;
  const u32 tagx = (tag >> 16 ^ tag6) & 63u, tagy = tag & 0xffffu;
  {
    const u32x4 cv = ldl<u32x4>(l0 + L_HC + 16 * lane);
    const u32 s1 = cv.x + cv.y, s2 = s1 + cv.z, s3 = s2 + cv.w;
    const u32 ex = wave_incl_scan(s3) - s3;
    const u32x4 st = {ex, ex + cv.x, ex + s1, ex + s2};
    stl<u32x4>(l0 + L_HS + 16 * lane, st);
    if (w < 4) {
      const __amdgpu_buffer_rsrc_t rtab = __builtin_amdgcn_make_buffer_rsrc((void*)(ws + W_TAB), 0, NCH * NBK * 8, 0x00020000);
      const u32 tv = w == 0 ? st.x | cv.x << 13 : w == 1 ? st.y | cv.y << 13 : w == 2 ? st.z | cv.z << 13 : st.w | cv.w << 13;
      __builtin_amdgcn_raw_buffer_store_b64(u32x2{tv | tag6 << 26, tag}, rtab, ((4 * lane + w) * NCH + c) * 8, 0, 16);
    }
  }
  {
    u32 sp[8];
#pragma unroll
    for (int j = 0; j < 8; ++j) sp[j] = ldl<u32>(l0 + L_HS + 4 * b[j]);
#pragma unroll
    for (int j = 0; j < 8; ++j) {
      const u32 eav = (j & 1) ? (a4[j >> 2][(j >> 1) & 1] & 0xffff0000u) : (a4[j >> 2][(j >> 1) & 1] << 16);
      const u32x2 rc = {s4[j >> 2][j & 3] | ((d4[j >> 2][j & 3] - umul24(b[j], NPB)) << 18 | tagx << 26), eav | tagy};
      if (j < 4 ? act0 : act1) stl<u32x2>(l0 + L_SREC + 8 * (sp[j] + r[j]), rc);
    }
  }
  LBAR();
  STAMP(4);
  const __amdgpu_buffer_rsrc_t rrec = __builtin_amdgcn_make_buffer_rsrc((void*)(ws + W_REC), 0, NCH * CS * 8, 0x00020000);
  {
    const int n = min(CS, E - c * CS);
    u32x4 cp[4];
#pragma unroll
    for (int i = 0; i < 4; ++i) cp[i] = ldl<u32x4>(l0 + L_SREC + 16 * (tid + 512 * i));
#pragma unroll
    for (int i = 0; i < 4; ++i) { const int q = tid + 512 * i;
      __builtin_amdgcn_raw_buffer_store_b128(cp[i], rrec, 2 * q < n ? (c * CS + 2 * q) * 8 : 0x7ffffff0, 0, 16); }
  }
  STAMP(5);
  const u32 r4 = (lane >> 2) & 3, cofs = 32 * ((lane >> 4) & 1) + 8 * (lane & 3);
  const u32 wtr = (8 * hh + r4) * 128 + cofs;
  bf16x8 W[8];
  {
    const u32 wa0_ = l0 + L_WRAW + wtr + 64 * (r4 >> 1), wa1_ = l0 + L_WRAW + wtr + 64 - 64 * (r4 >> 1);
    if (w < 7) {
      f32x16 A0, A1;
#pragma unroll
      for (int q = 0; q < 16; ++q) { A0[q] = 0.f; A1[q] = 0.f; }
#pragma unroll
      for (int s = 0; s < 4; ++s) {
        A0 = MFMA(cat8(ldtr(wa0_ + s * 2048), ldtr(wa0_ + s * 2048 + 512)), hb[s], A0);
        A1 = MFMA(cat8(ldtr(wa1_ + s * 2048), ldtr(wa1_ + s * 2048 + 512)), hb[s], A1);
      }
      const u32 nl = 32 * w + e32;
      if (nl <= NPB) {
        const u32 a = l0 + L_HM + nl * RS + 8 * hh;
#pragma unroll
        for (int q = 0; q < 4; ++q) {
          stl<u32x2>(a + 16 * q, u32x2{cvtpk(A0[4 * q], A0[4 * q + 1]), cvtpk(A0[4 * q + 2], A0[4 * q + 3])});
          stl<u32x2>(a + 64 + 16 * q, u32x2{cvtpk(A1[4 * q], A1[4 * q + 1]), cvtpk(A1[4 * q + 2], A1[4 * q + 3])});
        }
      }
    }
#pragma unroll
    for (int i = 0; i < 8; ++i) { const u32 a = ((i & 1) ? wa1_ : wa0_) + (4 + (i >> 1)) * 2048; W[i] = cat8(ldtr(a), ldtr(a + 512)); }
    if (tid < NPB) stl<f32x4>(l0 + L_PT + 16 * tid, f32x4{lo2f(ptile.x), hi2f(ptile.x), lo2f(ptile.y), 0.f});
  }
  STAMP(6);
  if (n0 >= N) return;

  u32 ent;
  {
    const char* tb = ws + W_TAB; const u32 vo = (bk * NCH + 32 * w + e32) * 8;
    u32x2 g; bool ok; u32 spins = 0;
    do {
      asm volatile("global_load_dwordx2 %0, %1, %2 sc1\n\ts_waitcnt vmcnt(0)" : "=&v"(g) : "v"(vo), "s"(tb) : "memory");
      ok = __builtin_amdgcn_ballot_w64(g.y != tag || (g.x >> 26) != tag6) == 0;
      if (!ok) __builtin_amdgcn_s_sleep(1);
    } while (!ok && ++spins < SPIN_MAX);
    ent = ok && lane < 32 ? g.x : 0u;
    if (!ok && lane == 0) stl<u32>(l0 + L_MISC + 36, 1u);
  }
  STAMP(7);
  u32 Tw, adr[8];
  {
    const u32 cn = (ent >> 13) & 0x1fff;
    u32 inc = cn; inc += dpp0<0x111>(inc); inc += dpp0<0x112>(inc); inc += dpp0<0x114>(inc); inc += dpp0<0x118>(inc); inc += dpp0<0x142, 0xa>(inc);
    Tw = __builtin_amdgcn_readlane(inc, 31);
    const u32 pre = inc - cn;
    const u32 delta = 8 * ((u32)(32 * w + lane) * (u32)CS + (ent & 0x1fff) - pre);
    const u32 dprev = (u32)__builtin_amdgcn_update_dpp(8, (int)delta, 0x138, 0xf, 0xf, false);
    if (lane < 32 && pre < 512) __hip_atomic_fetch_add((LAS u32*)(size_t)(lst + 4 * pre), delta - dprev, __ATOMIC_RELAXED, __HIP_MEMORY_SCOPE_WORKGROUP);
    const u32x4 m0 = ldl<u32x4>(lst + 32 * lane), m1 = ldl<u32x4>(lst + 32 * lane + 16);
    u32 a[8];
    a[0] = m0.x; a[1] = a[0] + m0.y; a[2] = a[1] + m0.z; a[3] = a[2] + m0.w; a[4] = a[3] + m1.x; a[5] = a[4] + m1.y; a[6] = a[5] + m1.z; a[7] = a[6] + m1.w;
    const u32 ex = wave_incl_scan(a[7]) - a[7];
    stl<u32x4>(lst + 32 * lane, u32x4{a[0] + ex, a[1] + ex, a[2] + ex, a[3] + ex});
    stl<u32x4>(lst + 32 * lane + 16, u32x4{a[4] + ex, a[5] + ex, a[6] + ex, a[7] + ex});
#pragma unroll
    for (int s = 0; s < 8; ++s) adr[s] = ldl<u32>(lst + 256 * s + 4 * lane);
  }
  if (Tw > 512) { if (lane == 0) stl<u32>(l0 + L_MISC + 36, 1u); Tw = 0; }
  const bool big = Tw > 448;
#define SLOTS(...) { _Pragma("unroll") for (int s = 0; s < 7; ++s) { __VA_ARGS__ } if (big) { constexpr int s = 7; __VA_ARGS__ } }
  bool v[8];
#pragma unroll
  for (int s = 0; s < 8; ++s) v[s] = (u32)(64 * s + lane) < Tw;
  u32x2 rec[8];
#define LDREC(off) __builtin_bit_cast(u32x2, __builtin_amdgcn_raw_buffer_load_b64(rrec, off, 0, 16))
  rec[7] = u32x2{0, 0};
  SLOTS(rec[s] = LDREC(v[s] ? adr[s] : 0x7ffffff0u);)
  STAMP(8);
  {
    u32 spins = 0;
    for (;;) {
      bool miss = false;
      SLOTS(miss |= v[s] && (((rec[s].x & 0xfc000000u) | (rec[s].y & 0xffffu)) != (tagx << 26 | tagy));)
      if (__builtin_amdgcn_ballot_w64(miss) == 0) break;
      if (++spins >= SPIN_MAX) { if (lane == 0) stl<u32>(l0 + L_MISC + 36, 1u); Tw = 0;
#pragma unroll
        for (int s = 0; s < 8; ++s) v[s] = false;
        break; }
      asm volatile("s_sleep 1" ::: "memory");
      SLOTS(rec[s] = LDREC(v[s] ? adr[s] : 0x7ffffff0u);)
    }
  }
  STAMP(9);
  const u32 tg = tagx << 10;
  u32 rank[8]; u32x2 ps[8];
  const u32 cntb = l0 + L_CNT - tg, six = 6;
  SLOTS(ps[s] = __builtin_bit_cast(u32x2, __builtin_amdgcn_raw_buffer_load_b64(rpos, mad16lo(rec[s].x, six, 0), 0, 0));
        if (v[s]) rank[s] = __hip_atomic_fetch_add((LAS u32*)(size_t)(cntb + (rec[s].x >> 16)), 1u, __ATOMIC_RELAXED, __HIP_MEMORY_SCOPE_WORKGROUP);)
  STAMP(10);
  LBAR();
  STAMP(11);
  if (blockIdx.x == 0 && tid == 0)
    __hip_atomic_store((gu64*)(ws + W_EP), (epv & 0xffffffff00000000ull) | tag, __ATOMIC_RELAXED, __HIP_MEMORY_SCOPE_AGENT);
  u32 wa0, wa1, wtb, wnt, wend; bool slow;
  {
    const u32x4 cv = ldl<u32x4>(l0 + L_CNT + 16 * lane);
    const u32 flag = ldl<u32>(l0 + L_MISC + 36);
    const u32 s1 = cv.x + cv.y, s2 = s1 + cv.z, s3 = s2 + cv.w;
    const u32 inc = wave_incl_scan(s3), ex = inc - s3;
    const u32 T = __builtin_amdgcn_readlane(inc, 63);
    const u32 off[4] = {ex, ex + cv.x, ex + s1, ex + s2};
    const float rT = T ? 8.f / (float)T : 0.f;
    u32 wv[4];
#pragma unroll
    for (int i = 0; i < 4; ++i) wv[i] = min(7u, (u32)((float)off[i] * rT));
    u32 prev = (u32)__builtin_amdgcn_update_dpp(-1, (int)wv[3], 0x138, 0xf, 0xf, false);
#pragma unroll
    for (int i = 0; i < 4; ++i) { const u32 n = 4 * lane + i;
      if (n < NPB && wv[i] != prev) stl<u32>(l0 + L_MISC + 4 * wv[i], off[i] << 8 | n);
      prev = wv[i]; }
    u32 cut = min(ldl<u32>(l0 + L_MISC + 4 * (lane & 7)), T << 8 | NPB);
    if (j16 >= 8) cut = T << 8 | NPB;
    cut = min(cut, (u32)__builtin_amdgcn_update_dpp(-1, (int)cut, 0x101, 0xf, 0xf, false));
    cut = min(cut, (u32)__builtin_amdgcn_update_dpp(-1, (int)cut, 0x102, 0xf, 0xf, false));
    cut = min(cut, (u32)__builtin_amdgcn_update_dpp(-1, (int)cut, 0x104, 0xf, 0xf, false));
    const u32 nxt = (u32)__builtin_amdgcn_update_dpp(-1, (int)cut, 0x101, 0xf, 0xf, false);
    u32 len = j16 < 8 ? (nxt >> 8) - (cut >> 8) : 0, nt = (len + 31) >> 5;
    u32 tb = nt; tb += dpp0<0x111>(tb); tb += dpp0<0x112>(tb); tb += dpp0<0x114>(tb);
    const u32 ttot = __builtin_amdgcn_readlane(tb, 7);
    tb -= nt;
    const u32 delta = 32 * tb - (cut >> 8);
#pragma unroll
    for (int i = 0; i < 4; ++i) { const u32 o = off[i] + (u32)__builtin_amdgcn_ds_bpermute(4 * wv[i], (int)delta);
      if (lane < NPB / 4) stl<u32>(l0 + L_PT + 64 * lane + 16 * i + 12, o); }
    wa0 = __builtin_amdgcn_readlane(cut, w) & 0xff; wa1 = __builtin_amdgcn_readlane(nxt, w) & 0xff;
    wtb = __builtin_amdgcn_readlane(tb, w); wnt = __builtin_amdgcn_readlane(nt, w);
    wend = 32 * wtb + __builtin_amdgcn_readlane(len, w);
    slow = ttot > MAXT || __builtin_amdgcn_readfirstlane(flag) != 0;
  }
  if (!slow) {
  STAMP(12);
  u32 pp[8]; f32x4 pd[8];
  const u32 ptb = l0 + L_PT - 4 * tg, four = 4;
  SLOTS(pd[s] = ldl<f32x4>(mad16hi(rec[s].x, four, ptb));)
  SLOTS(pp[s] = l0 + L_R + 16 * (__float_as_uint(pd[s].w) + rank[s]); if (v[s]) stl<u32>(pp[s] + 12, rec[s].x);)
  {
    const u32 p = wend + lane;
    if (p < 32 * (wtb + wnt)) stl<u32x4>(l0 + L_R + 16 * p, u32x4{0, 0, 0, (4 * NPB + tg) << 16});
  }
  LBAR();
  STAMP(13);
  const Dma dh = {(const char*)h, (const char*)h - 1024, (const char*)h - 2048, (const char*)h - 3072};
  const u32 slot0 = l0 + L_STG + w * 8192;
  const u32 dch0 = 16 * ((lane & 7) ^ (lane >> 4)), dch1 = dch0 ^ 64;
  const u32 sraddr = l0 + L_R + 512 * wtb + 16 * (lane >> 3) + 12;
  const u32 c128 = 128;
  auto ldsrc = [&](u32 a) { return u32x4{ldl<u32>(a), ldl<u32>(a + 128), ldl<u32>(a + 256), ldl<u32>(a + 384)}; };
  auto issue = [&](u32 a, u32 b, u32x4 sv, u32 slot) {
    dma4_if(a, b, mad16lo(sv.x, c128, dch0), mad16lo(sv.y, c128, dch1), mad16lo(sv.z, c128, dch0), mad16lo(sv.w, c128, dch1), dh, slot);
  };
  SLOTS(asm volatile("" : "+v"(ps[s].x), "+v"(ps[s].y));)
  issue(0, wnt, ldsrc(sraddr), slot0);
  issue(1, wnt, ldsrc(sraddr + 512), slot0 + 4096);
  SLOTS(const float dx = pd[s].x - lo2f(ps[s].x); const float dy = pd[s].y - hi2f(ps[s].x); const float dz = pd[s].z - lo2f(ps[s].y);
        const float d2 = dx * dx + dy * dy + dz * dz;
        const u32 de_ = __builtin_amdgcn_perm(rec[s].y, cvtpk(d2, 0.f), 0x07060100u);
        if (v[s]) { stl<u32x2>(pp[s], u32x2{cvtpk(1.f, dx), cvtpk(dy, dz)}); stl<u32>(pp[s] + 8, de_); })
  STAMP(14);
  LBAR();
  STAMP(15);

  const u32 swz = (e32 >> 1) & 7;
  u32 sa[4];
#pragma unroll
  for (int s = 0; s < 4; ++s) sa[s] = slot0 + e32 * 128 + 16 * ((2 * s + hh) ^ swz);
  const u32 eq = 4 * hh + ((lane >> 2) & 3), slotA = 4 * ((lane >> 4) & 1) + (lane & 3);
  u32 zv = l0 + L_Z; asm volatile("" : "+v"(zv));
  u32 a_de = l0 + L_R + 512 * wtb + 16 * e32 + 8;
  u32 a_v = l0 + L_R + 512 * wtb + 16 * eq;
  u32 a_sr = sraddr + 1536;
  const u32 ohb = l0 + L_OH + (hh ? 144 : 0), ohl = hh ? 24 : 32, nh8 = (u32)(-32 * hh), dem = hh ? 0xffffffffu : 0u;
  const u32 pA = l0 + L_HM + (8 * hh + r4) * RS + cofs - (RS / 4) * tg;
  const bool spc = hh && r4 >= 2;
  const u32 pB = spc ? l0 + L_WR + (r4 - 2) * 128 + cofs : pA + 4 * RS;
  u32 pM = spc ? 0 : 1; asm volatile("" : "+v"(pM));
  f32x16 Y0, Y1, X0, X1, ZERO;
#pragma unroll
  for (int r = 0; r < 16; ++r) { Y0[r] = 0.f; Y1[r] = 0.f; ZERO[r] = 0.f; }
  u32 base = wa0, tgt = 4 * (wa0 + slotA) + tg;
  int bad = 0;
  const u32 fn = (lane & 31) >> 2, fi = lane & 3;
  u32 f_wx = l0 + L_WX + 16 * hh, f_m = l0 + L_HM + fn * RS + 8 * hh, f_p = l0 + L_PA + 16 * fn + 4 * (fi - 1);
  asm volatile("" : "+v"(f_wx), "+v"(f_m), "+v"(f_p));
  auto flush = [&]() {
    u32x4 c[4];
#pragma unroll
    for (int i = 0; i < 4; ++i) { c[0][i] = cvtpk(Y0[2 * i], Y0[2 * i + 1]); c[1][i] = cvtpk(Y0[8 + 2 * i], Y0[9 + 2 * i]);
                                  c[2][i] = cvtpk(Y1[2 * i], Y1[2 * i + 1]); c[3][i] = cvtpk(Y1[8 + 2 * i], Y1[9 + 2 * i]); }
    f32x16 D = ZERO;
#pragma unroll
    for (int i = 0; i < 4; ++i) D = MFMA(ldl<bf16x8>(f_wx + 32 * i), __builtin_bit_cast(bf16x8, c[i]), D);
    const u32 node = base + fn;
    if (node < wa1) {
      if (fi == 0) {
        const u32 a = f_m + base * RS;
#pragma unroll
        for (int q = 0; q < 4; ++q) {
          stl<u32x2>(a + 16 * q, u32x2{c[q >> 1][2 * (q & 1)], c[q >> 1][2 * (q & 1) + 1]});
          stl<u32x2>(a + 64 + 16 * q, u32x2{c[2 + (q >> 1)][2 * (q & 1)], c[2 + (q >> 1)][2 * (q & 1) + 1]});
        }
      } else if (hh == 0) stl<float>(f_p + 16 * base, D[0] * -LN2);
    }
  };
  if (wnt > 0) {
    bf16x8 As[4], Pb0, Pb1; u32x4 OH; u32x4 P0[2], P1[2];
    u32 lastc, lastn, aoh, aA, aB;
    u32 lastreal;
    lastreal = __builtin_amdgcn_readfirstlane(ldl<u32>(l0 + L_R + 16 * (wend - 1) + 12) >> 16);
    float tt[4];
    auto front = [&](u32 dsb, u32 valid) {
      const u32 first = __builtin_amdgcn_readlane(dsb, 0); lastn = min((u32)__builtin_amdgcn_readlane(dsb, 31), lastreal);
      const u32 wb = min(first, (u32)(4 * (NPB - XW)) + tg);
      bad = max(bad, (int)((lastn - wb) & valid));
      aoh = ohb + 4 * min(dsb - wb + nh8, ohl);
      const u32 wo = wb * (RS / 4);
      aA = pA + wo; aB = umul24(pM, wo) + pB;
    };
    auto rdwin = [&]() { OH = ldl<u32x4>(aoh);
      Pb0 = cat8(ldtr(aA), ldtr(aB)); Pb1 = cat8(ldtr(aA + 64), ldtr(aB + 64)); };
#define EA(X, b) { _Pragma("unroll") for (int k_ = 0; k_ < 4; ++k_) tt[k_] = __builtin_amdgcn_exp2f(X[b + k_]); SB(); \
                   _Pragma("unroll") for (int k_ = 0; k_ < 4; ++k_) tt[k_] = 1.f + tt[k_]; SB(); }
#define RMC(X, b, P) { _Pragma("unroll") for (int k_ = 0; k_ < 4; ++k_) tt[k_] = __builtin_amdgcn_rcpf(tt[k_]); SB(); \
                   _Pragma("unroll") for (int k_ = 0; k_ < 4; ++k_) tt[k_] = vmul(tt[k_], X[b + k_]); SB(); \
                   P[(b) >> 3][((b) >> 1) & 3] = cvtpk(tt[0], tt[1]); P[(b) >> 3][(((b) >> 1) & 3) + 1] = cvtpk(tt[2], tt[3]); SB(); }
#define BF(x) __builtin_bit_cast(bf16x8, x)
    {
      const u32x2 ds2 = ldl<u32x2>(a_de); const u32 de = ds2.x, dsb = ds2.y >> 16; const u32x4 sv = ldsrc(a_sr - 512);
      wait_dma(1, wnt);
#pragma unroll
      for (int s = 0; s < 4; ++s) As[s] = ldl<bf16x8>(sa[s]);
      front(dsb, ~0u); lastc = (lastn - tg) >> 2;
      rdwin();
      asm volatile("s_waitcnt lgkmcnt(0)" ::: "memory");
      issue(2, wnt, sv, slot0);
      OH.w |= de & dem;
      X0 = MFMA(As[0], W[0], ZERO); X0 = MFMA(As[1], W[2], X0); X0 = MFMA(As[2], W[4], X0); X0 = MFMA(As[3], W[6], X0); X0 = MFMA(BF(OH), Pb0, X0);
    }
    auto body = [&](auto uc, u32 t) {
      constexpr int U = decltype(uc)::value;
      const u32x4 dsw = ldsrc(a_v + 12 + 512 * U);
      const u32x2 ds2 = ldl<u32x2>(a_de + 512 * (U + 1)); const u32 de = ds2.x, dsb = ds2.y >> 16;
      const u32x4 sv = ldsrc(a_sr + 512 * U);
      SB();
      X1 = MFMA(BF(OH), Pb1, ZERO); SB();
      EA(X0, 0)
      X1 = MFMA(As[0], W[1], X1); SB();
      RMC(X0, 0, P0)
      X1 = MFMA(As[1], W[3], X1); SB();
      EA(X0, 4)
      X1 = MFMA(As[2], W[5], X1); SB();
      RMC(X0, 4, P0)
      X1 = MFMA(As[3], W[7], X1); SB();
      wait_dma(t + 2, wnt);
#pragma unroll
      for (int s = 0; s < 4; ++s) As[s] = ldl<bf16x8>(sa[s] + 4096 * (U ^ 1));
      SB();
      front(dsb, t + 1 < wnt ? ~0u : 0u);
      u32 sel[4];
#pragma unroll
      for (int a = 0; a < 4; ++a) sel[a] = (dsw[a] >> 16) == tgt ? a_v : zv;
      SB();
      EA(X0, 8) RMC(X0, 8, P0)
      asm volatile("s_waitcnt lgkmcnt(0)" ::: "memory");
      issue(t + 3, wnt, sv, slot0 + 4096 * (U ^ 1));
      rdwin();
      s16x4 Bq[4];
#pragma unroll
      for (int a = 0; a < 4; ++a) Bq[a] = ldtr(sel[a] + 128 * a + 512 * U);
      SB();
      EA(X0, 12) RMC(X0, 12, P0)
      X0 = MFMA(As[0], W[0], ZERO); SB();
      EA(X1, 0)
      X0 = MFMA(As[1], W[2], X0); SB();
      RMC(X1, 0, P1)
      X0 = MFMA(As[2], W[4], X0); SB();
      EA(X1, 4)
      X0 = MFMA(As[3], W[6], X0); SB();
      RMC(X1, 4, P1)
      OH.w |= de & dem; SB();
      X0 = MFMA(BF(OH), Pb0, X0); SB();
      EA(X1, 8)
      bf16x8 B0 = cat8(Bq[0], Bq[1]), B1 = cat8(Bq[2], Bq[3]);
      Y0 = MFMA(BF(P0[0]), B0, Y0); SB();
      RMC(X1, 8, P1)
      Y0 = MFMA(BF(P0[1]), B1, Y0); SB();
      EA(X1, 12)
      Y1 = MFMA(BF(P1[0]), B0, Y1); SB();
      RMC(X1, 12, P1)
      Y1 = MFMA(BF(P1[1]), B1, Y1); SB();
      if (lastc >= base + 8) do {
        flush(); base += 8; tgt += 32;
        asm volatile("" : "+s"(base), "+v"(tgt));
#pragma unroll
        for (int a = 0; a < 4; ++a) Bq[a] = ldtr(((dsw[a] >> 16) == tgt ? a_v : zv) + 128 * a + 512 * U);
        B0 = cat8(Bq[0], Bq[1]); B1 = cat8(Bq[2], Bq[3]);
        Y0 = MFMA(BF(P0[0]), B0, ZERO); Y1 = MFMA(BF(P1[0]), B0, ZERO);
        Y0 = MFMA(BF(P0[1]), B1, Y0); Y1 = MFMA(BF(P1[1]), B1, Y1);
      } while (lastc >= base + 8);
      lastc = (lastn - tg) >> 2;
    };
    for (u32 t = 0;; t += 2) {
      body(IC<0>{}, t);
      if (t + 1 >= wnt) break;
      body(IC<1>{}, t + 1);
      if (t + 2 >= wnt) break;
      a_de += 1024; a_v += 1024; a_sr += 1024;
    }
  }
  STAMP(16);
  {
    const u32 rb = (u32)(n0 + 32 * w) + (lane >> 3), nm = (u32)N - 1;
    dma4(min(rb, nm) * 128 + dch0, min(rb + 8, nm) * 128 + dch1, min(rb + 16, nm) * 128 + dch0, min(rb + 24, nm) * 128 + dch1, dh, slot0);
    u32 sv; const char* g = (const char*)wh + w * 2048; const u32 vo = (lane * 16) ^ ((lane >> 4 & 1) << 6);
    asm volatile("s_mov_b32 %0, m0\n\ts_mov_b32 m0, %3\n\ts_nop 0\n\tglobal_load_lds_dwordx4 %1, %2\n\tglobal_load_lds_dwordx4 %1, %2 offset:1024\n\ts_mov_b32 m0, %0"
                 : "=&s"(sv) : "v"(vo), "s"(g), "s"(slot0 + 4096) : "memory");
  }
  if (base < wa1) { flush(); base += 8; }
  if (base < wa1) {
#pragma unroll
    for (int r = 0; r < 16; ++r) { Y0[r] = 0.f; Y1[r] = 0.f; }
    do { flush(); base += 8; } while (base < wa1);
  }
  if (bad >= 4 * XW && lane == 0) stl<u32>(l0 + L_MISC + 36, 1u);
  STAMP(17);
  asm volatile("s_waitcnt vmcnt(0) lgkmcnt(0)\n\ts_barrier" ::: "memory");
  STAMP(18);
  slow = __builtin_amdgcn_readfirstlane(ldl<u32>(l0 + L_MISC + 36)) != 0;

  if (!slow) {
  if (w < 7) {
    const u32 nl = 32 * w + e32, mrow = l0 + L_HM + min(nl, (u32)NPB) * RS + 16 * hh;
    f32x16 A0, A1, C0, C1;
#pragma unroll
    for (int r = 0; r < 16; ++r) { A0[r] = 0.f; A1[r] = 0.f; C0[r] = 0.f; C1[r] = 0.f; }
#pragma unroll
    for (int s = 0; s < 4; ++s) {
      const bf16x8 hv = ldl<bf16x8>(sa[s]), mb = ldl<bf16x8>(mrow + 32 * s);
      const u32 wa = l0 + L_STG + 4096 + wtr + 64 * (r4 >> 1), wb = l0 + L_STG + 4096 + wtr + 64 - 64 * (r4 >> 1);
      const bf16x8 w0 = cat8(ldtr(wa + s * 8192), ldtr(wa + s * 8192 + 512)), w1 = cat8(ldtr(wb + s * 8192), ldtr(wb + s * 8192 + 512));
      const bf16x8 w2 = cat8(ldtr(wa + (4 + s) * 8192), ldtr(wa + (4 + s) * 8192 + 512)), w3 = cat8(ldtr(wb + (4 + s) * 8192), ldtr(wb + (4 + s) * 8192 + 512));
      A0 = MFMA(w0, hv, A0); A1 = MFMA(w1, hv, A1); C0 = MFMA(w2, mb, C0); C1 = MFMA(w3, mb, C1);
    }
#pragma unroll
    for (int ft = 0; ft < 2; ++ft)
#pragma unroll
      for (int q = 0; q < 4; ++q) {
        const u32 a = slot0 + e32 * 128 + 16 * ((4 * ft + q) ^ swz) + 8 * hh;
        const u32x2 hv = ldl<u32x2>(a);
        float o[4];
#pragma unroll
        for (int k = 0; k < 4; ++k) { const float z = ft ? A1[4 * q + k] - LN2 * C1[4 * q + k] : A0[4 * q + k] - LN2 * C0[4 * q + k]; o[k] = silu_f(z); }
        stl<u32x2>(a, u32x2{cvtpk(lo2f(hv.x) + o[0], hi2f(hv.x) + o[1]), cvtpk(lo2f(hv.y) + o[2], hi2f(hv.y) + o[3])});
      }
    asm volatile("s_waitcnt lgkmcnt(0)" ::: "memory");
#pragma unroll
    for (int a = 0; a < 4; ++a) {
      const u32 row = 8 * a + (lane >> 3), nr = 32 * w + row;
      const u32x4 v = ldl<u32x4>(slot0 + a * 1024 + lane * 16);
      if (nr < NPB && n0 + nr < (u32)N) *(u32x4*)((char*)out + (size_t)(n0 + nr) * 128 + ((a & 1) ? dch1 : dch0)) = v;
    }
  } else {
    const int nn = min(NPB, N - n0);
    u32* po = (u32*)(out + (size_t)N * 64 + (size_t)n0 * 3);
    for (int i = lane; 2 * i < 3 * nn; i += 64) {
      const int e0 = 2 * i, e1 = e0 + 1, a0 = e0 / 3, a1 = e1 / 3, c0 = e0 - 3 * a0, c1 = e1 - 3 * a1;
      const float v0 = ldl<float>(l0 + L_PT + 16 * a0 + 4 * c0) + ldl<float>(l0 + L_PA + 16 * a0 + 4 * c0);
      const float v1 = ldl<float>(l0 + L_PT + 16 * a1 + 4 * c1) + ldl<float>(l0 + L_PA + 16 * a1 + 4 * c1);
      po[i] = cvtpk(v0, v1);
    }
  }
  STAMP(19);
  }
  }
  if (slow) slow_bucket(h, pos, ea, src, dst, we, wx, wh, out, N, E, bk, l0);
  STAMP_DUMP(160u << 20, bk * 8 + w);
}

__global__ void gen_zero(float* p, int n) { int i = blockIdx.x * blockDim.x + threadIdx.x; if (i < n) p[i] = 0.f; }
__global__ void gen_edge(const u16* h, const u16* pos, const u16* ea, const int* src, const int* dst, const u16* we, const u16* wx, float* mi, float* pd, int E) {
  const int lane = threadIdx.x & 63, w = threadIdx.x >> 6, nw = blockDim.x >> 6;
  const float wxf = bf2f(wx[lane]);
  for (int e = blockIdx.x * nw + w; e < E; e += gridDim.x * nw) {
    const int s = src[e], d = dst[e];
    float df[3], d2 = 0.f;
#pragma unroll
    for (int k = 0; k < 3; ++k) { df[k] = lo2f(cvtpk(bf2f(pos[(size_t)d * 3 + k]) - bf2f(pos[(size_t)s * 3 + k]), 0.f)); d2 += df[k] * df[k]; }
    d2 = lo2f(cvtpk(d2, 0.f));
    const float hd = bf2f(h[(size_t)d * 64 + lane]), hs = bf2f(h[(size_t)s * 64 + lane]);
    float z = 0.f;
#pragma unroll 4
    for (int k = 0; k < 64; ++k) z += __shfl(hd, k) * bf2f(we[k * 64 + lane]) + __shfl(hs, k) * bf2f(we[(64 + k) * 64 + lane]);
    z += d2 * bf2f(we[128 * 64 + lane]) + bf2f(ea[e]) * bf2f(we[129 * 64 + lane]);
    const float m = lo2f(cvtpk(silu_f(z), 0.f));
    float cs = m * wxf;
#pragma unroll
    for (int o = 32; o > 0; o >>= 1) cs += __shfl_xor(cs, o);
    atomicAdd(&mi[(size_t)d * 64 + lane], m);
    if (lane < 3) atomicAdd(&pd[(size_t)d * 3 + lane], (lane == 0 ? df[0] : lane == 1 ? df[1] : df[2]) * cs);
  }
}
__global__ void gen_node(const u16* h, const u16* pos, const u16* wh, const float* mi, const float* pd, u16* out, int N) {
  const int lane = threadIdx.x & 63, w = threadIdx.x >> 6, nw = blockDim.x >> 6;
  for (int n = blockIdx.x * nw + w; n < N; n += gridDim.x * nw) {
    const float hv = bf2f(h[(size_t)n * 64 + lane]), mv = lo2f(cvtpk(mi[(size_t)n * 64 + lane], 0.f));
    float z = 0.f;
#pragma unroll 4
    for (int k = 0; k < 64; ++k) z += __shfl(hv, k) * bf2f(wh[k * 64 + lane]) + __shfl(mv, k) * bf2f(wh[(64 + k) * 64 + lane]);
    out[(size_t)n * 64 + lane] = (u16)cvtpk(hv + silu_f(z), 0.f);
    if (lane < 3) out[(size_t)N * 64 + (size_t)n * 3 + lane] = (u16)cvtpk(bf2f(pos[(size_t)n * 3 + lane]) + pd[(size_t)n * 3 + lane], 0.f);
  }
}

extern "C" void kernel_launch(void* const* d_in, const int* in_sizes, int n_in, void* d_out, int out_size, void* d_ws, size_t ws_size, hipStream_t stream) {
  const u16* h = (const u16*)d_in[0];
  const u16* pos = (const u16*)d_in[1];
  const u16* ea = (const u16*)d_in[2];
  const int* src = (const int*)d_in[3];
  const int* dst = (const int*)d_in[4];
  const u16* we = (const u16*)d_in[5];
  const u16* wx = (const u16*)d_in[6];
  const u16* wh = (const u16*)d_in[7];
  const int N = in_sizes[0] / 64, E = in_sizes[3];
  const int CS = ((E + NCH - 1) / NCH + 3) & ~3;
  const bool fast = N >= 2 && N <= NPB * NBK && (N * 6) % 64 != 0 && (E & 3) == 0 && E > 0 && CS <= 4096 && W_REC + (size_t)NCH * CS * 8 <= ws_size;
  if (fast) {
    (void)hipFuncSetAttribute((const void*)egnn_fused, hipFuncAttributeMaxDynamicSharedMemorySize, L_END);
    egnn_fused<<<NBK, 512, L_END, stream>>>(h, pos, ea, src, dst, we, wx, wh, (char*)d_ws, (u16*)d_out, N, E, CS);
  } else {
    float* mi = (float*)d_ws; float* pd = mi + (size_t)N * 64;
    const int nz = N * 67;
    gen_zero<<<(nz + 255) / 256, 256, 0, stream>>>(mi, nz);
    gen_edge<<<2048, 256, 0, stream>>>(h, pos, ea, src, dst, we, wx, mi, pd, E);
    gen_node<<<1024, 256, 0, stream>>>(h, pos, wh, mi, pd, (u16*)d_out, N);
  }
}
```

```cpp
#include <hip/hip_runtime.h>
#include <hip/hip_bf16.h>
#include <stdint.h>

typedef unsigned short u16;
typedef unsigned u32;
typedef short bf16x8 __attribute__((ext_vector_type(8)));
typedef short s16x4 __attribute__((ext_vector_type(4)));
typedef float f32x16 __attribute__((ext_vector_type(16)));
typedef float f32x4 __attribute__((ext_vector_type(4)));
typedef unsigned u32x4 __attribute__((ext_vector_type(4)));
typedef unsigned u32x2 __attribute__((ext_vector_type(2)));
#define LAS __attribute__((address_space(3)))

#define HBPOS 1
constexpr int NPB = 196;
constexpr int NBK = 256;
constexpr int NCH = 256;
constexpr int MAXT = 112;
constexpr float LOG2E = 1.4426950408889634f, LN2 = 0.6931471805599453f;

constexpr int NT1 = MAXT + 1;
constexpr int RS = 144;
constexpr int XW = 14;
constexpr int L_STG = 0;
constexpr int L_R = 65536;
constexpr int L_HM = L_R + NT1 * 512;
constexpr int L_PT = L_HM + 6 * 32 * RS + 1024;
constexpr int L_PA = L_PT + NPB * 16;
constexpr int L_CNT = L_PA + NPB * 16;
constexpr int L_WX = L_CNT + 1024;
constexpr int L_WR = L_WX + 128;
constexpr int L_OH = L_WR + 256;
constexpr int L_Z = L_OH + 256;
constexpr int L_MISC = L_Z + 1024;
constexpr int L_END = L_MISC + 64;
constexpr int L_SREC = L_STG;
constexpr int L_WRAW = L_STG + 32768;
constexpr int L_HC = L_STG + 49152;
constexpr int L_HS = L_HC + 1024;
static_assert(L_HM % 16 == 0 && L_PT % 16 == 0 && L_CNT % 16 == 0 && L_Z % 16 == 0 && L_WX % 16 == 0 && L_OH % 16 == 0, "align");
static_assert(L_END <= 160 * 1024, "lds");

constexpr size_t W_EP = 0;
constexpr size_t W_TAB = 4096;
constexpr size_t W_REC = W_TAB + NCH * NBK * 8;

static __device__ __forceinline__ float bf2f(u32 v) { return __uint_as_float(v << 16); }
typedef __bf16 bf16x2_t __attribute__((ext_vector_type(2)));
typedef float f32x2 __attribute__((ext_vector_type(2)));
static __device__ __forceinline__ u32 cvtpk(float lo, float hi) { return __builtin_bit_cast(u32, __builtin_convertvector(f32x2{lo, hi}, bf16x2_t)); }
static __device__ __forceinline__ float lo2f(u32 v) { return __uint_as_float(v << 16); }
static __device__ __forceinline__ float hi2f(u32 v) { return __uint_as_float(v & 0xffff0000u); }
template <int CTRL, int RM = 0xf> static __device__ __forceinline__ u32 dpp0(u32 x) { return (u32)__builtin_amdgcn_update_dpp(0, (int)x, CTRL, RM, 0xf, true); }
static __device__ __forceinline__ u32 wave_incl_scan(u32 x) {
  x += dpp0<0x111>(x); x += dpp0<0x112>(x); x += dpp0<0x114>(x); x += dpp0<0x118>(x);
  x += dpp0<0x142, 0xa>(x); x += dpp0<0x143, 0xc>(x); return x; }
#define MFMA(a, b, c) __builtin_amdgcn_mfma_f32_32x32x16_bf16(a, b, c, 0, 0, 0)
#define SB() __builtin_amdgcn_sched_barrier(0)
#define LBAR() asm volatile("s_waitcnt lgkmcnt(0)\n\ts_barrier" ::: "memory")
template <class T> static __device__ __forceinline__ T ldl(u32 a) { return *(LAS const T*)(size_t)a; }
template <class T> static __device__ __forceinline__ void stl(u32 a, T v) { *(LAS T*)(size_t)a = v; }
static __device__ __forceinline__ s16x4 ldtr(u32 a) { typedef short v4 __attribute__((ext_vector_type(4)));
  return __builtin_bit_cast(s16x4, __builtin_amdgcn_ds_read_tr16_b64_v4i16((LAS v4*)(size_t)a)); }
static __device__ __forceinline__ float silu_f(float z) { return z * __builtin_amdgcn_rcpf(1.f + __builtin_amdgcn_exp2f(-LOG2E * z)); }

#define STAMP(i)
#define STAMP_DECL
#define STAMP_DUMP(base, idx)
struct Dma { const char* b0; const char* b1; const char* b2; const char* b3; };
static __device__ __forceinline__ void dma4(u32 v0, u32 v1, u32 v2, u32 v3, const Dma& d, u32 lds) {
  u32 sv;
  asm volatile("s_mov_b32 %0, m0\n\ts_mov_b32 m0, %9\n\ts_nop 0\n\t"
               "global_load_lds_dwordx4 %1, %5\n\t"
               "global_load_lds_dwordx4 %2, %6 offset:1024\n\t"
               "global_load_lds_dwordx4 %3, %7 offset:2048\n\t"
               "global_load_lds_dwordx4 %4, %8 offset:3072\n\t"
               "s_mov_b32 m0, %0"
               : "=&s"(sv) : "v"(v0), "v"(v1), "v"(v2), "v"(v3), "s"(d.b0), "s"(d.b1), "s"(d.b2), "s"(d.b3), "s"(lds) : "memory");
}

static __device__ __forceinline__ void slow_bucket(const u16* __restrict__ h, const u16* __restrict__ pos, const u16* __restrict__ ea, const int* __restrict__ src,
                                                const int* __restrict__ dst, const u16* __restrict__ we, const u16* __restrict__ wx,
                                                const u16* __restrict__ wh, u16* __restrict__ out, int N, int E, int bk, u32 l0) {
  const int tid = threadIdx.x, lane = tid & 63, w = tid >> 6, n0 = bk * NPB;
  LAS float* macc = (LAS float*)(size_t)(l0 + L_STG);
  LAS float* pacc = (LAS float*)(size_t)(l0 + L_PA);
  __syncthreads();
  for (int i = tid; i < NPB * 64; i += 512) macc[i] = 0.f;
  for (int i = tid; i < NPB * 4; i += 512) pacc[i] = 0.f;
  __syncthreads();
  const float wxf = bf2f(wx[lane]);
  for (int eb = 64 * w; eb < E; eb += 512) {
    const int dv = eb + lane < E ? dst[eb + lane] : -1;
    unsigned long long m = __builtin_amdgcn_ballot_w64((u32)(dv - n0) < (u32)NPB && dv >= 0);
    while (m) {
      const int l = __builtin_ctzll(m); m &= m - 1;
      const int e = eb + l, d = __builtin_amdgcn_readlane(dv, l), s = src[e], dl = d - n0;
      float df[3], d2 = 0.f;
#pragma unroll
      for (int k = 0; k < 3; ++k) { df[k] = lo2f(cvtpk(bf2f(pos[d * 3 + k]) - bf2f(pos[s * 3 + k]), 0.f)); d2 += df[k] * df[k]; }
      d2 = lo2f(cvtpk(d2, 0.f));
      const float hd = bf2f(h[(size_t)d * 64 + lane]), hs = bf2f(h[(size_t)s * 64 + lane]);
      float z = 0.f;
#pragma unroll 4
      for (int k = 0; k < 64; ++k) z += __shfl(hd, k) * bf2f(we[k * 64 + lane]) + __shfl(hs, k) * bf2f(we[(64 + k) * 64 + lane]);
      z += d2 * bf2f(we[128 * 64 + lane]) + bf2f(ea[e]) * bf2f(we[129 * 64 + lane]);
      const float mm = lo2f(cvtpk(silu_f(z), 0.f));
      float cs = mm * wxf;
#pragma unroll
      for (int o = 32; o > 0; o >>= 1) cs += __shfl_xor(cs, o);
      atomicAdd((float*)&macc[dl * 64 + lane], mm);
      if (lane < 3) atomicAdd((float*)&pacc[dl * 4 + lane], (lane == 0 ? df[0] : lane == 1 ? df[1] : df[2]) * cs);
    }
  }
  __syncthreads();
  for (int nl = w; nl < NPB; nl += 8) {
    const int n = n0 + nl; if (n >= N) break;
    const float hv = bf2f(h[(size_t)n * 64 + lane]), mv = lo2f(cvtpk(macc[nl * 64 + lane], 0.f));
    float z = 0.f;
#pragma unroll 4
    for (int k = 0; k < 64; ++k) z += __shfl(hv, k) * bf2f(wh[k * 64 + lane]) + __shfl(mv, k) * bf2f(wh[(64 + k) * 64 + lane]);
    out[(size_t)n * 64 + lane] = (u16)cvtpk(hv + silu_f(z), 0.f);
    if (lane < 3) out[(size_t)N * 64 + (size_t)n * 3 + lane] = (u16)cvtpk(bf2f(pos[n * 3 + lane]) + pacc[nl * 4 + lane], 0.f);
  }
}
static __device__ __forceinline__ u32 umul24(u32 a, u32 b) { return (u32)__umul24(a, b); }
static __device__ __forceinline__ u32 mad16lo(u32 a, u32 b, u32 c) { u32 r; asm("v_mad_u32_u16 %0, %1, %2, %3" : "=v"(r) : "v"(a), "s"(b), "v"(c)); return r; }
static __device__ __forceinline__ u32 mad16hi(u32 a, u32 b, u32 c) { u32 r; asm("v_mad_u32_u16 %0, %1, %2, %3 op_sel:[1,0,0,0]" : "=v"(r) : "v"(a), "s"(b), "v"(c)); return r; }
static __device__ __forceinline__ bf16x8 cat8(s16x4 a, s16x4 b) { return __builtin_shufflevector(a, b, 0, 1, 2, 3, 4, 5, 6, 7); }
extern "C" __device__ float llvm_fmul_legacy(float, float) __asm("llvm.amdgcn.fmul.legacy");
static __device__ __forceinline__ float vmul(float a, float b) { return llvm_fmul_legacy(a, b); }
static __device__ __forceinline__ void wait_dma(u32 a, u32 b) {
  asm volatile("s_cmp_lt_u32 %0, %1\n\ts_cbranch_scc1 1f\n\ts_waitcnt vmcnt(0)\n1:\n\ts_waitcnt vmcnt(4)" :: "s"(a), "s"(b) : "scc", "memory"); }
static __device__ __forceinline__ void dma4_if(u32 a, u32 b, u32 v0, u32 v1, u32 v2, u32 v3, const Dma& d, u32 lds) {
  u32 sv;
  asm volatile("s_cmp_lt_u32 %10, %11\n\ts_cbranch_scc0 1f\n\t"
               "s_mov_b32 %0, m0\n\ts_mov_b32 m0, %9\n\ts_nop 0\n\t"
               "global_load_lds_dwordx4 %1, %5\n\t"
               "global_load_lds_dwordx4 %2, %6 offset:1024\n\t"
               "global_load_lds_dwordx4 %3, %7 offset:2048\n\t"
               "global_load_lds_dwordx4 %4, %8 offset:3072\n\t"
               "s_mov_b32 m0, %0\n1:"
               : "=&s"(sv) : "v"(v0), "v"(v1), "v"(v2), "v"(v3), "s"(d.b0), "s"(d.b1), "s"(d.b2), "s"(d.b3), "s"(lds), "s"(a), "s"(b) : "scc", "memory");
}
template <int V> struct IC { static constexpr int value = V; };

constexpr u32 SPIN_MAX = 1u << 15;
typedef __attribute__((address_space(1))) unsigned long long gu64;

__global__ void __launch_bounds__(512, 2) egnn_fused(const u16* __restrict__ h, const u16* __restrict__ pos, const u16* __restrict__ ea, const int* __restrict__ src,
                                                     const int* __restrict__ dst, const u16* __restrict__ we, const u16* __restrict__ wx, const u16* __restrict__ wh,
                                                     char* __restrict__ ws, u16* __restrict__ out, int N, int E, int CS) {
  extern __shared__ __attribute__((aligned(16))) char smem[];
  const u32 l0 = (u32)(size_t)(LAS char*)smem;
  const int tid = threadIdx.x, lane = tid & 63;
  const int w = __builtin_amdgcn_readfirstlane(tid >> 6);
  const int c = blockIdx.x;
  const int bk = (blockIdx.x & 7) * 32 + (blockIdx.x >> 3);
  const int n0 = bk * NPB;
  STAMP_DECL STAMP(0);

  const int grp = lane >> 4, j16 = lane & 15, e32 = lane & 31, hh = lane >> 5;
  const int e0 = c * CS + 4 * tid;
  const bool act0 = 4 * tid < CS && e0 < E, act1 = 4 * tid + 2048 < CS && e0 + 2048 < E;
  const __amdgpu_buffer_rsrc_t rdst = __builtin_amdgcn_make_buffer_rsrc((void*)dst, 0, E * 4, 0x00020000), rsrc_ = __builtin_amdgcn_make_buffer_rsrc((void*)src, 0, E * 4, 0x00020000);
  const __amdgpu_buffer_rsrc_t rea = __builtin_amdgcn_make_buffer_rsrc((void*)ea, 0, E * 2, 0x00020000);
  const u32 eo0 = act0 ? (u32)e0 : 0x1fffff00u, eo1 = act1 ? (u32)e0 + 2048 : 0x1fffff00u;
  u32x4 d4[2], s4[2]; u32x2 a4[2];
  d4[0] = __builtin_bit_cast(u32x4, __builtin_amdgcn_raw_buffer_load_b128(rdst, eo0 * 4, 0, 0));
  d4[1] = __builtin_bit_cast(u32x4, __builtin_amdgcn_raw_buffer_load_b128(rdst, eo1 * 4, 0, 0));
  s4[0] = __builtin_bit_cast(u32x4, __builtin_amdgcn_raw_buffer_load_b128(rsrc_, eo0 * 4, 0, 0));
  s4[1] = __builtin_bit_cast(u32x4, __builtin_amdgcn_raw_buffer_load_b128(rsrc_, eo1 * 4, 0, 0));
  a4[0] = __builtin_bit_cast(u32x2, __builtin_amdgcn_raw_buffer_load_b64(rea, eo0 * 2, 0, 0));
  a4[1] = __builtin_bit_cast(u32x2, __builtin_amdgcn_raw_buffer_load_b64(rea, eo1 * 2, 0, 0));
  const unsigned long long epv = __hip_atomic_load((gu64*)(ws + W_EP), __ATOMIC_RELAXED, __HIP_MEMORY_SCOPE_AGENT);
  u32x4 wt[2];
#pragma unroll
  for (int i = 0; i < 2; ++i) wt[i] = ((const u32x4*)we)[tid + 512 * i];
  const u32 wxr = wx[16 * ((tid >> 4) & 3) + (tid & 3) + 8 * ((tid >> 2) & 1) + 4 * ((tid >> 3) & 1)];
  const u32 wrr = we[128 * 64 + (tid & 127)];
  const int nmax = N - 1;
  const __amdgpu_buffer_rsrc_t rpos = __builtin_amdgcn_make_buffer_rsrc((void*)pos, 0, N * 6 + 2, 0x00020000);
  const u32x2 ptile = __builtin_bit_cast(u32x2, __builtin_amdgcn_raw_buffer_load_b64(rpos, min(n0 + tid, nmax) * 6, 0, 0));
  const Dma dh = {(const char*)h, (const char*)h - 1024, (const char*)h - 2048, (const char*)h - 3072};
  const u32 dch0 = 16 * ((lane & 7) ^ (lane >> 4)), dch1 = dch0 ^ 64;
  const u32 hbs = l0 + L_HM + 32 * RS * w;
  auto dma_rows = [&](u32 slot) {
    const u32 rb = (u32)(n0 + 32 * w) + (lane >> 3), nm = (u32)nmax;
    dma4(min(rb, nm) * 128 + dch0, min(rb + 8, nm) * 128 + dch1, min(rb + 16, nm) * 128 + dch0, min(rb + 24, nm) * 128 + dch1, dh, slot); };
  auto dma_hb = [&]() {
    if (w < 6) dma_rows(hbs);
    else if (w == 6) { u32 sv; const u32 vo = min((u32)(n0 + 192) + (lane >> 3), (u32)nmax) * 128 + dch0;
      asm volatile("s_mov_b32 %0, m0\n\ts_mov_b32 m0, %3\n\ts_nop 0\n\tglobal_load_lds_dwordx4 %1, %2\n\ts_mov_b32 m0, %0" : "=&s"(sv) : "v"(vo), "s"(h), "s"(hbs) : "memory"); } };
  if (tid < 256) { stl<u32>(l0 + L_CNT + 4 * tid, 0u); stl<u32>(l0 + L_HC + 4 * tid, 0u); }
  if (tid < 16) stl<u32>(l0 + L_MISC + 4 * tid, tid < 8 ? 0xffffffffu : 0u);
  if (tid < 256) stl<u32>(l0 + L_Z + 4 * tid, 0u);
  const u32 lst = l0 + L_R + 2048 * w;
  stl<u32x4>(lst + 32 * lane, u32x4{8, 8, 8, 8}); stl<u32x4>(lst + 32 * lane + 16, u32x4{8, 8, 8, 8});
  if (tid < NPB) stl<f32x4>(l0 + L_PA + 16 * tid, f32x4{0, 0, 0, 0});
  if (tid < 64) { const int u = tid < 36 ? tid : tid - 36, e = u >> 2, lim = tid < 36 ? 8 : 6;
    stl<u32>(l0 + L_OH + 4 * tid, (e < lim && (e >> 1) == (u & 3)) ? ((e & 1) ? 0x3f800000u : 0x3f80u) : 0u); }
  STAMP(22);
  LBAR();
  STAMP(1);
  STAMP(2);
  u32 b[8], r[8];
#pragma unroll
  for (int j = 0; j < 8; ++j) b[j] = umul24(d4[j >> 2][j & 3], 85599u) >> 24;
  if (act0) {
#pragma unroll
    for (int j = 0; j < 4; ++j) r[j] = __hip_atomic_fetch_add((LAS u32*)(size_t)(l0 + L_HC + 4 * b[j]), 1u, __ATOMIC_RELAXED, __HIP_MEMORY_SCOPE_WORKGROUP);
  }
  if (act1) {
#pragma unroll
    for (int j = 4; j < 8; ++j) r[j] = __hip_atomic_fetch_add((LAS u32*)(size_t)(l0 + L_HC + 4 * b[j]), 1u, __ATOMIC_RELAXED, __HIP_MEMORY_SCOPE_WORKGROUP);
  }
#pragma unroll
  for (int i = 0; i < 2; ++i) {
    const u32 q = tid + 512 * i;
    u32x4 o;
#pragma unroll
    for (int k = 0; k < 4; ++k) o[k] = cvtpk(lo2f(wt[i][k]) * -LOG2E, hi2f(wt[i][k]) * -LOG2E);
    stl<u32x4>(l0 + L_WRAW + ((q * 16) ^ ((q >> 4 & 1) << 6)), o);
  }
  if (tid < 64) stl<u16>(l0 + L_WX + 2 * tid, (u16)wxr);
  if (tid < 128) stl<u16>(l0 + L_WR + 2 * tid, (u16)cvtpk(bf2f(wrr) * -LOG2E, 0.f));
  if (tid < NPB) stl<f32x4>(l0 + L_PT + 16 * tid, f32x4{lo2f(ptile.x), hi2f(ptile.x), lo2f(ptile.y), 0.f});
  asm volatile("" :: "v"(s4[0]), "v"(s4[1]), "v"(a4[0]), "v"(a4[1]), "v"(ptile), "v"(epv));
  LBAR();
  STAMP(3);
  const u32 tag = __builtin_amdgcn_readfirstlane((u32)epv) + 1u, tag6 = __builtin_amdgcn_readfirstlane((u32)(epv >> 32)) & 63u;
  const u32 tagx = (tag >> 16 ^ tag6) & 63u, tagy = tag & 0xffffu;
  {
    const u32x4 cv = ldl<u32x4>(l0 + L_HC + 16 * lane);
    const u32 s1 = cv.x + cv.y, s2 = s1 + cv.z, s3 = s2 + cv.w;
    const u32 ex = wave_incl_scan(s3) - s3;
    const u32x4 st = {ex, ex + cv.x, ex + s1, ex + s2};
    stl<u32x4>(l0 + L_HS + 16 * lane, st);
    if (w < 4) {
      const __amdgpu_buffer_rsrc_t rtab = __builtin_amdgcn_make_buffer_rsrc((void*)(ws + W_TAB), 0, NCH * NBK * 8, 0x00020000);
      const u32 tv = w == 0 ? st.x | cv.x << 13 : w == 1 ? st.y | cv.y << 13 : w == 2 ? st.z | cv.z << 13 : st.w | cv.w << 13;
      __builtin_amdgcn_raw_buffer_store_b64(u32x2{tv | tag6 << 26, tag}, rtab, ((4 * lane + w) * NCH + c) * 8, 0, 16);
    }
  }
  {
    u32 sp[8];
#pragma unroll
    for (int j = 0; j < 8; ++j) sp[j] = ldl<u32>(l0 + L_HS + 4 * b[j]);
#pragma unroll
    for (int j = 0; j < 8; ++j) {
      const u32 eav = (j & 1) ? (a4[j >> 2][(j >> 1) & 1] & 0xffff0000u) : (a4[j >> 2][(j >> 1) & 1] << 16);
      const u32x2 rc = {s4[j >> 2][j & 3] | ((d4[j >> 2][j & 3] - umul24(b[j], NPB)) << 18 | tagx << 26), eav | tagy};
      if (j < 4 ? act0 : act1) stl<u32x2>(l0 + L_SREC + 8 * (sp[j] + r[j]), rc);
    }
  }
  LBAR();
  STAMP(4);
  const __amdgpu_buffer_rsrc_t rrec = __builtin_amdgcn_make_buffer_rsrc((void*)(ws + W_REC), 0, NCH * CS * 8, 0x00020000);
  {
    const int n = min(CS, E - c * CS);
    u32x4 cp[4];
#pragma unroll
    for (int i = 0; i < 4; ++i) cp[i] = ldl<u32x4>(l0 + L_SREC + 16 * (tid + 512 * i));
#pragma unroll
    for (int i = 0; i < 4; ++i) { const int q = tid + 512 * i;
      __builtin_amdgcn_raw_buffer_store_b128(cp[i], rrec, 2 * q < n ? (c * CS + 2 * q) * 8 : 0x7ffffff0, 0, 16); }
  }
  dma_hb();
  STAMP(5);
  const u32 r4 = (lane >> 2) & 3, cofs = 32 * ((lane >> 4) & 1) + 8 * (lane & 3);
  const u32 wtr = (8 * hh + r4) * 128 + cofs;
  bf16x8 W[8], W1[8];
  {
    const u32 wa0_ = l0 + L_WRAW + wtr + 64 * (r4 >> 1), wa1_ = l0 + L_WRAW + wtr + 64 - 64 * (r4 >> 1);
#pragma unroll
    for (int i = 0; i < 8; ++i) { const u32 a = ((i & 1) ? wa1_ : wa0_) + (i >> 1) * 2048; W1[i] = cat8(ldtr(a), ldtr(a + 512)); W[i] = cat8(ldtr(a + 8192), ldtr(a + 8192 + 512)); }
  }
  STAMP(6);
  if (n0 >= N) { asm volatile("s_waitcnt vmcnt(0)" ::: "memory"); return; }

  u32 ent;
  {
    const char* tb = ws + W_TAB; const u32 vo = (bk * NCH + 32 * w + e32) * 8;
    u32x2 g; bool ok; u32 spins = 0;
    do {
      asm volatile("global_load_dwordx2 %0, %1, %2 sc1\n\ts_waitcnt vmcnt(0)" : "=&v"(g) : "v"(vo), "s"(tb) : "memory");
      ok = __builtin_amdgcn_ballot_w64(g.y != tag || (g.x >> 26) != tag6) == 0;
      if (!ok) __builtin_amdgcn_s_sleep(1);
    } while (!ok && ++spins < SPIN_MAX);
    ent = ok && lane < 32 ? g.x : 0u;
    if (!ok) stl<u32>(l0 + L_MISC + 36, 1u);
  }
  STAMP(7);
  u32 Tw, adr[8];
  {
    const u32 cn = (ent >> 13) & 0x1fff;
    u32 inc = cn; inc += dpp0<0x111>(inc); inc += dpp0<0x112>(inc); inc += dpp0<0x114>(inc); inc += dpp0<0x118>(inc); inc += dpp0<0x142, 0xa>(inc);
    Tw = __builtin_amdgcn_readlane(inc, 31);
    const u32 pre = inc - cn;
    const u32 delta = 8 * ((u32)(32 * w + lane) * (u32)CS + (ent & 0x1fff) - pre);
    const u32 dprev = (u32)__builtin_amdgcn_update_dpp(8, (int)delta, 0x138, 0xf, 0xf, false);
    if (lane < 32 && pre < 512) __hip_atomic_fetch_add((LAS u32*)(size_t)(lst + 4 * pre), delta - dprev, __ATOMIC_RELAXED, __HIP_MEMORY_SCOPE_WORKGROUP);
    const u32x4 m0 = ldl<u32x4>(lst + 32 * lane), m1 = ldl<u32x4>(lst + 32 * lane + 16);
    u32 a[8];
    a[0] = m0.x; a[1] = a[0] + m0.y; a[2] = a[1] + m0.z; a[3] = a[2] + m0.w; a[4] = a[3] + m1.x; a[5] = a[4] + m1.y; a[6] = a[5] + m1.z; a[7] = a[6] + m1.w;
    const u32 ex = wave_incl_scan(a[7]) - a[7];
    stl<u32x4>(lst + 32 * lane, u32x4{a[0] + ex, a[1] + ex, a[2] + ex, a[3] + ex});
    stl<u32x4>(lst + 32 * lane + 16, u32x4{a[4] + ex, a[5] + ex, a[6] + ex, a[7] + ex});
#pragma unroll
    for (int s = 0; s < 8; ++s) adr[s] = ldl<u32>(lst + 256 * s + 4 * lane);
  }
  if (Tw > 512) { stl<u32>(l0 + L_MISC + 36, 1u); Tw = 0; }
  const bool big = Tw > 448;
#define SLOTS(...) { _Pragma("unroll") for (int s = 0; s < 7; ++s) { __VA_ARGS__ } if (big) { constexpr int s = 7; __VA_ARGS__ } }
#define V(s) ((u32)(64 * (s) + lane) < Tw)
  u32x2 rec[8];
#define LDREC(off) __builtin_bit_cast(u32x2, __builtin_amdgcn_raw_buffer_load_b64(rrec, off, 0, 16))
#pragma unroll
  for (int s = 0; s < 8; ++s) { adr[s] = V(s) ? adr[s] : 0x7ffffff0u; rec[s] = LDREC(adr[s]); }
  const u32 swz = (e32 >> 1) & 7;
  if (w < 7) {
    f32x16 A0, A1;
#pragma unroll
    for (int q = 0; q < 16; ++q) { A0[q] = 0.f; A1[q] = 0.f; }
#pragma unroll
    for (int s = 0; s < 4; ++s) { const bf16x8 hb = ldl<bf16x8>(hbs + e32 * 128 + 16 * ((2 * s + hh) ^ swz)); A0 = MFMA(W1[2 * s], hb, A0); A1 = MFMA(W1[2 * s + 1], hb, A1); }
    const u32 nl = 32 * w + e32;
    if (nl <= NPB) {
      const u32 a = l0 + L_HM + nl * RS + 8 * hh;
#pragma unroll
      for (int q = 0; q < 4; ++q) {
        stl<u32x2>(a + 16 * q, u32x2{cvtpk(A0[4 * q], A0[4 * q + 1]), cvtpk(A0[4 * q + 2], A0[4 * q + 3])});
        stl<u32x2>(a + 64 + 16 * q, u32x2{cvtpk(A1[4 * q], A1[4 * q + 1]), cvtpk(A1[4 * q + 2], A1[4 * q + 3])});
      }
    }
  }
  __builtin_amdgcn_sched_barrier(0);
  STAMP(8);
  {
    const u32 tagw = tagx << 26 | tagy;
#define BAD(s) (__builtin_amdgcn_ballot_w64(((rec[s].x & 0xfc000000u) | (rec[s].y & 0xffffu)) != tagw) & __builtin_amdgcn_ballot_w64(V(s)))
    unsigned long long miss = 0;
    SLOTS(miss |= BAD(s);)
    if (miss != 0) {
      u32 spins = 0;
      do {
        asm volatile("s_sleep 1" ::: "memory");
#pragma unroll
        for (int s = 0; s < 8; ++s) rec[s] = LDREC(adr[s]);
        miss = 0;
#pragma unroll
        for (int s = 0; s < 8; ++s) miss |= BAD(s);
      } while (miss != 0 && ++spins < SPIN_MAX);
      if (miss != 0) { stl<u32>(l0 + L_MISC + 36, 1u); Tw = 0; }
    }
  }
  STAMP(9);
  const u32 tg = tagx << 10;
  u32 rank[8]; u32x2 ps[8];
  const u32 cntb = l0 + L_CNT - tg, six = 6;
#pragma unroll
  for (int s = 0; s < 8; ++s) ps[s] = __builtin_bit_cast(u32x2, __builtin_amdgcn_raw_buffer_load_b64(rpos, mad16lo(rec[s].x, six, 0), 0, 0));
  SLOTS(if (V(s)) rank[s] = __hip_atomic_fetch_add((LAS u32*)(size_t)(cntb + (rec[s].x >> 16)), 1u, __ATOMIC_RELAXED, __HIP_MEMORY_SCOPE_WORKGROUP);)
  STAMP(10);
  LBAR();
  STAMP(11);
  if (blockIdx.x == 0 && tid == 0)
    __hip_atomic_store((gu64*)(ws + W_EP), (epv & 0xffffffff00000000ull) | tag, __ATOMIC_RELAXED, __HIP_MEMORY_SCOPE_AGENT);
  u32 wa0, wa1, wtb, wnt, wend; bool slow;
  {
    const u32x4 cv = ldl<u32x4>(l0 + L_CNT + 16 * lane);
    const u32 flag = ldl<u32>(l0 + L_MISC + 36);
    const u32 s1 = cv.x + cv.y, s2 = s1 + cv.z, s3 = s2 + cv.w;
    const u32 inc = wave_incl_scan(s3), ex = inc - s3;
    const u32 T = __builtin_amdgcn_readlane(inc, 63);
    const u32 off[4] = {ex, ex + cv.x, ex + s1, ex + s2};
    const float rT = T ? 8.f / (float)T : 0.f;
    u32 wv[4];
#pragma unroll
    for (int i = 0; i < 4; ++i) wv[i] = min(7u, (u32)((float)off[i] * rT));
    u32 prev = (u32)__builtin_amdgcn_update_dpp(-1, (int)wv[3], 0x138, 0xf, 0xf, false);
#pragma unroll
    for (int i = 0; i < 4; ++i) { const u32 n = 4 * lane + i;
      if (n < NPB && wv[i] != prev) stl<u32>(l0 + L_MISC + 4 * wv[i], off[i] << 8 | n);
      prev = wv[i]; }
    u32 cut = min(ldl<u32>(l0 + L_MISC + 4 * (lane & 7)), T << 8 | NPB);
    if (j16 >= 8) cut = T << 8 | NPB;
    cut = min(cut, (u32)__builtin_amdgcn_update_dpp(-1, (int)cut, 0x101, 0xf, 0xf, false));
    cut = min(cut, (u32)__builtin_amdgcn_update_dpp(-1, (int)cut, 0x102, 0xf, 0xf, false));
    cut = min(cut, (u32)__builtin_amdgcn_update_dpp(-1, (int)cut, 0x104, 0xf, 0xf, false));
    const u32 nxt = (u32)__builtin_amdgcn_update_dpp(-1, (int)cut, 0x101, 0xf, 0xf, false);
    u32 len = j16 < 8 ? (nxt >> 8) - (cut >> 8) : 0, nt = (len + 31) >> 5;
    u32 tb = nt; tb += dpp0<0x111>(tb); tb += dpp0<0x112>(tb); tb += dpp0<0x114>(tb);
    const u32 ttot = __builtin_amdgcn_readlane(tb, 7);
    tb -= nt;
    const u32 delta = 32 * tb - (cut >> 8);
#pragma unroll
    for (int i = 0; i < 4; ++i) { const u32 o = off[i] + (u32)__builtin_amdgcn_ds_bpermute(4 * wv[i], (int)delta);
      if (lane < NPB / 4) stl<u32>(l0 + L_PT + 64 * lane + 16 * i + 12, o); }
    wa0 = __builtin_amdgcn_readlane(cut, w) & 0xff; wa1 = __builtin_amdgcn_readlane(nxt, w) & 0xff;
    wtb = __builtin_amdgcn_readlane(tb, w); wnt = __builtin_amdgcn_readlane(nt, w);
    wend = 32 * wtb + __builtin_amdgcn_readlane(len, w);
    slow = ttot > MAXT || __builtin_amdgcn_readfirstlane(flag) != 0;
  }
  if (!slow) {
  STAMP(12);
  u32 pp[8]; f32x4 pd[8];
  const u32 ptb = l0 + L_PT - 4 * tg, four = 4;
  SLOTS(pd[s] = ldl<f32x4>(mad16hi(rec[s].x, four, ptb));)
  SLOTS(pp[s] = l0 + L_R + 16 * (__float_as_uint(pd[s].w) + rank[s]); if (V(s)) stl<u32>(pp[s] + 12, rec[s].x);)
  {
    const u32 p = wend + lane;
    if (p < 32 * (wtb + wnt)) stl<u32x4>(l0 + L_R + 16 * p, u32x4{0, 0, 0, (4 * NPB + tg) << 16});
  }
  LBAR();
  STAMP(13);
  const u32 slot0 = l0 + L_STG + w * 8192;
  const u32 sraddr = l0 + L_R + 512 * wtb + 16 * (lane >> 3) + 12;
  const u32 c128 = 128;
  auto ldsrc = [&](u32 a) { return u32x4{ldl<u32>(a), ldl<u32>(a + 128), ldl<u32>(a + 256), ldl<u32>(a + 384)}; };
  auto issue = [&](u32 a, u32 b, u32x4 sv, u32 slot) {
    dma4_if(a, b, mad16lo(sv.x, c128, dch0), mad16lo(sv.y, c128, dch1), mad16lo(sv.z, c128, dch0), mad16lo(sv.w, c128, dch1), dh, slot);
  };
  SLOTS(asm volatile("" : "+v"(ps[s].x), "+v"(ps[s].y));)
  issue(0, wnt, ldsrc(sraddr), slot0);
  issue(1, wnt, ldsrc(sraddr + 512), slot0 + 4096);
  SLOTS(const float dx = pd[s].x - lo2f(ps[s].x); const float dy = pd[s].y - hi2f(ps[s].x); const float dz = pd[s].z - lo2f(ps[s].y);
        const float d2 = dx * dx + dy * dy + dz * dz;
        const u32 de_ = __builtin_amdgcn_perm(rec[s].y, cvtpk(d2, 0.f), 0x07060100u);
        if (V(s)) { stl<u32x2>(pp[s], u32x2{cvtpk(1.f, dx), cvtpk(dy, dz)}); stl<u32>(pp[s] + 8, de_); })
  STAMP(14);
  LBAR();
  STAMP(15);

  u32 sa[4];
#pragma unroll
  for (int s = 0; s < 4; ++s) sa[s] = slot0 + e32 * 128 + 16 * ((2 * s + hh) ^ swz);
  const u32 eq = 4 * hh + ((lane >> 2) & 3), slotA = 4 * ((lane >> 4) & 1) + (lane & 3);
  u32 zv = l0 + L_Z; asm volatile("" : "+v"(zv));
  u32 a_de = l0 + L_R + 512 * wtb + 16 * e32 + 8;
  u32 a_v = l0 + L_R + 512 * wtb + 16 * eq;
  u32 a_sr = sraddr + 1536;
  const u32 ohb = l0 + L_OH + (hh ? 144 : 0), ohl = hh ? 24 : 32, nh8 = (u32)(-32 * hh), dem = hh ? 0xffffffffu : 0u;
  const u32 pA = l0 + L_HM + (8 * hh + r4) * RS + cofs - (RS / 4) * tg;
  const bool spc = hh && r4 >= 2;
  const u32 pB = spc ? l0 + L_WR + (r4 - 2) * 128 + cofs : pA + 4 * RS;
  u32 pM = spc ? 0 : 1; asm volatile("" : "+v"(pM));
  f32x16 Y0, Y1, X0, X1, ZERO;
#pragma unroll
  for (int r = 0; r < 16; ++r) { Y0[r] = 0.f; Y1[r] = 0.f; ZERO[r] = 0.f; }
  u32 base = wa0, tgt = 4 * (wa0 + slotA) + tg;
  int bad = 0;
  const u32 fn = (lane & 31) >> 2, fi = lane & 3;
  u32 f_wx = l0 + L_WX + 16 * hh, f_m = l0 + L_HM + fn * RS + 8 * hh, f_p = l0 + L_PA + 16 * fn + 4 * (fi - 1);
  asm volatile("" : "+v"(f_wx), "+v"(f_m), "+v"(f_p));
  auto flush = [&]() {
    u32x4 c[4];
#pragma unroll
    for (int i = 0; i < 4; ++i) { c[0][i] = cvtpk(Y0[2 * i], Y0[2 * i + 1]); c[1][i] = cvtpk(Y0[8 + 2 * i], Y0[9 + 2 * i]);
                                  c[2][i] = cvtpk(Y1[2 * i], Y1[2 * i + 1]); c[3][i] = cvtpk(Y1[8 + 2 * i], Y1[9 + 2 * i]); }
    f32x16 D = ZERO;
#pragma unroll
    for (int i = 0; i < 4; ++i) D = MFMA(ldl<bf16x8>(f_wx + 32 * i), __builtin_bit_cast(bf16x8, c[i]), D);
    const u32 node = base + fn;
    if (node < wa1) {
      if (fi == 0) {
        const u32 a = f_m + base * RS;
#pragma unroll
        for (int q = 0; q < 4; ++q) {
          stl<u32x2>(a + 16 * q, u32x2{c[q >> 1][2 * (q & 1)], c[q >> 1][2 * (q & 1) + 1]});
          stl<u32x2>(a + 64 + 16 * q, u32x2{c[2 + (q >> 1)][2 * (q & 1)], c[2 + (q >> 1)][2 * (q & 1) + 1]});
        }
      } else if (hh == 0) stl<float>(f_p + 16 * base, D[0] * -LN2);
    }
  };
  if (wnt > 0) {
    bf16x8 As[4], Pb0, Pb1; u32x4 OH; u32x4 P0[2], P1[2];
    u32 lastc, lastn, aoh, aA, aB;
    u32 lastreal;
    lastreal = __builtin_amdgcn_readfirstlane(ldl<u32>(l0 + L_R + 16 * (wend - 1) + 12) >> 16);
    float tt[4];
    auto front = [&](u32 dsb, u32 valid) {
      const u32 first = __builtin_amdgcn_readlane(dsb, 0); lastn = min((u32)__builtin_amdgcn_readlane(dsb, 31), lastreal);
      const u32 wb = min(first, (u32)(4 * (NPB - XW)) + tg);
      bad = max(bad, (int)((lastn - wb) & valid));
      aoh = ohb + 4 * min(dsb - wb + nh8, ohl);
      const u32 wo = wb * (RS / 4);
      aA = pA + wo; aB = umul24(pM, wo) + pB;
    };
    auto rdwin = [&]() { OH = ldl<u32x4>(aoh);
      Pb0 = cat8(ldtr(aA), ldtr(aB)); Pb1 = cat8(ldtr(aA + 64), ldtr(aB + 64)); };
#define EA(X, b) { _Pragma("unroll") for (int k_ = 0; k_ < 4; ++k_) tt[k_] = __builtin_amdgcn_exp2f(X[b + k_]); SB(); \
                   _Pragma("unroll") for (int k_ = 0; k_ < 4; ++k_) tt[k_] = 1.f + tt[k_]; SB(); }
#define RMC(X, b, P) { _Pragma("unroll") for (int k_ = 0; k_ < 4; ++k_) tt[k_] = __builtin_amdgcn_rcpf(tt[k_]); SB(); \
                   _Pragma("unroll") for (int k_ = 0; k_ < 4; ++k_) tt[k_] = vmul(tt[k_], X[b + k_]); SB(); \
                   P[(b) >> 3][((b) >> 1) & 3] = cvtpk(tt[0], tt[1]); P[(b) >> 3][(((b) >> 1) & 3) + 1] = cvtpk(tt[2], tt[3]); SB(); }
#define BF(x) __builtin_bit_cast(bf16x8, x)
    {
      const u32x2 ds2 = ldl<u32x2>(a_de); const u32 de = ds2.x, dsb = ds2.y >> 16; const u32x4 sv = ldsrc(a_sr - 512);
      wait_dma(1, wnt);
#pragma unroll
      for (int s = 0; s < 4; ++s) As[s] = ldl<bf16x8>(sa[s]);
      front(dsb, ~0u); lastc = (lastn - tg) >> 2;
      rdwin();
      asm volatile("s_waitcnt lgkmcnt(0)" ::: "memory");
      issue(2, wnt, sv, slot0);
      OH.w |= de & dem;
      X0 = MFMA(As[0], W[0], ZERO); X0 = MFMA(As[1], W[2], X0); X0 = MFMA(As[2], W[4], X0); X0 = MFMA(As[3], W[6], X0); X0 = MFMA(BF(OH), Pb0, X0);
    }
    auto body = [&](auto uc, u32 t) {
      constexpr int U = decltype(uc)::value;
      const u32x4 dsw = ldsrc(a_v + 12 + 512 * U);
      const u32x2 ds2 = ldl<u32x2>(a_de + 512 * (U + 1)); const u32 de = ds2.x, dsb = ds2.y >> 16;
      const u32x4 sv = ldsrc(a_sr + 512 * U);
      SB();
      X1 = MFMA(BF(OH), Pb1, ZERO); SB();
      EA(X0, 0)
      X1 = MFMA(As[0], W[1], X1); SB();
      RMC(X0, 0, P0)
      X1 = MFMA(As[1], W[3], X1); SB();
      EA(X0, 4)
      X1 = MFMA(As[2], W[5], X1); SB();
      RMC(X0, 4, P0)
      X1 = MFMA(As[3], W[7], X1); SB();
      wait_dma(t + 2, wnt);
#pragma unroll
      for (int s = 0; s < 4; ++s) As[s] = ldl<bf16x8>(sa[s] + 4096 * (U ^ 1));
      SB();
      front(dsb, t + 1 < wnt ? ~0u : 0u);
      u32 sel[4];
#pragma unroll
      for (int a = 0; a < 4; ++a) sel[a] = (dsw[a] >> 16) == tgt ? a_v : zv;
      SB();
      EA(X0, 8) RMC(X0, 8, P0)
      asm volatile("s_waitcnt lgkmcnt(0)" ::: "memory");
      issue(t + 3, wnt, sv, slot0 + 4096 * (U ^ 1));
      rdwin();
      s16x4 Bq[4];
#pragma unroll
      for (int a = 0; a < 4; ++a) Bq[a] = ldtr(sel[a] + 128 * a + 512 * U);
      SB();
      EA(X0, 12) RMC(X0, 12, P0)
      X0 = MFMA(As[0], W[0], ZERO); SB();
      EA(X1, 0)
      X0 = MFMA(As[1], W[2], X0); SB();
      RMC(X1, 0, P1)
      X0 = MFMA(As[2], W[4], X0); SB();
      EA(X1, 4)
      X0 = MFMA(As[3], W[6], X0); SB();
      RMC(X1, 4, P1)
      OH.w |= de & dem; SB();
      X0 = MFMA(BF(OH), Pb0, X0); SB();
      EA(X1, 8)
      bf16x8 B0 = cat8(Bq[0], Bq[1]), B1 = cat8(Bq[2], Bq[3]);
      Y0 = MFMA(BF(P0[0]), B0, Y0); SB();
      RMC(X1, 8, P1)
      Y0 = MFMA(BF(P0[1]), B1, Y0); SB();
      EA(X1, 12)
      Y1 = MFMA(BF(P1[0]), B0, Y1); SB();
      RMC(X1, 12, P1)
      Y1 = MFMA(BF(P1[1]), B1, Y1); SB();
      if (lastc >= base + 8) do {
        flush(); base += 8; tgt += 32;
        asm volatile("" : "+s"(base), "+v"(tgt));
#pragma unroll
        for (int a = 0; a < 4; ++a) Bq[a] = ldtr(((dsw[a] >> 16) == tgt ? a_v : zv) + 128 * a + 512 * U);
        B0 = cat8(Bq[0], Bq[1]); B1 = cat8(Bq[2], Bq[3]);
        Y0 = MFMA(BF(P0[0]), B0, ZERO); Y1 = MFMA(BF(P1[0]), B0, ZERO);
        Y0 = MFMA(BF(P0[1]), B1, Y0); Y1 = MFMA(BF(P1[1]), B1, Y1);
      } while (lastc >= base + 8);
      lastc = (lastn - tg) >> 2;
    };
    for (u32 t = 0;; t += 2) {
      body(IC<0>{}, t);
      if (t + 1 >= wnt) break;
      body(IC<1>{}, t + 1);
      if (t + 2 >= wnt) break;
      a_de += 1024; a_v += 1024; a_sr += 1024;
    }
  }
  STAMP(16);
  {
    dma_rows(slot0);
    u32 sv; const char* g = (const char*)wh + w * 2048; const u32 vo = (lane * 16) ^ ((lane >> 4 & 1) << 6);
    asm volatile("s_mov_b32 %0, m0\n\ts_mov_b32 m0, %3\n\ts_nop 0\n\tglobal_load_lds_dwordx4 %1, %2\n\tglobal_load_lds_dwordx4 %1, %2 offset:1024\n\ts_mov_b32 m0, %0"
                 : "=&s"(sv) : "v"(vo), "s"(g), "s"(slot0 + 4096) : "memory");
  }
  if (base < wa1) { flush(); base += 8; }
  if (base < wa1) {
#pragma unroll
    for (int r = 0; r < 16; ++r) { Y0[r] = 0.f; Y1[r] = 0.f; }
    do { flush(); base += 8; } while (base < wa1);
  }
  if (bad >= 4 * XW && lane == 0) stl<u32>(l0 + L_MISC + 36, 1u);
  STAMP(17);
  asm volatile("s_waitcnt vmcnt(0) lgkmcnt(0)\n\ts_barrier" ::: "memory");
  STAMP(18);
  slow = __builtin_amdgcn_readfirstlane(ldl<u32>(l0 + L_MISC + 36)) != 0;

  if (!slow) {
  if (w < 7) {
    const u32 nl = 32 * w + e32, mrow = l0 + L_HM + min(nl, (u32)NPB) * RS + 16 * hh;
    f32x16 A0, A1, C0, C1;
#pragma unroll
    for (int r = 0; r < 16; ++r) { A0[r] = 0.f; A1[r] = 0.f; C0[r] = 0.f; C1[r] = 0.f; }
#pragma unroll
    for (int s = 0; s < 4; ++s) {
      const bf16x8 hv = ldl<bf16x8>(sa[s]), mb = ldl<bf16x8>(mrow + 32 * s);
      const u32 wa = l0 + L_STG + 4096 + wtr + 64 * (r4 >> 1), wb = l0 + L_STG + 4096 + wtr + 64 - 64 * (r4 >> 1);
      const bf16x8 w0 = cat8(ldtr(wa + s * 8192), ldtr(wa + s * 8192 + 512)), w1 = cat8(ldtr(wb + s * 8192), ldtr(wb + s * 8192 + 512));
      const bf16x8 w2 = cat8(ldtr(wa + (4 + s) * 8192), ldtr(wa + (4 + s) * 8192 + 512)), w3 = cat8(ldtr(wb + (4 + s) * 8192), ldtr(wb + (4 + s) * 8192 + 512));
      A0 = MFMA(w0, hv, A0); A1 = MFMA(w1, hv, A1); C0 = MFMA(w2, mb, C0); C1 = MFMA(w3, mb, C1);
    }
#pragma unroll
    for (int ft = 0; ft < 2; ++ft)
#pragma unroll
      for (int q = 0; q < 4; ++q) {
        const u32 a = slot0 + e32 * 128 + 16 * ((4 * ft + q) ^ swz) + 8 * hh;
        const u32x2 hv = ldl<u32x2>(a);
        float o[4];
#pragma unroll
        for (int k = 0; k < 4; ++k) { const float z = ft ? A1[4 * q + k] - LN2 * C1[4 * q + k] : A0[4 * q + k] - LN2 * C0[4 * q + k]; o[k] = silu_f(z); }
        stl<u32x2>(a, u32x2{cvtpk(lo2f(hv.x) + o[0], hi2f(hv.x) + o[1]), cvtpk(lo2f(hv.y) + o[2], hi2f(hv.y) + o[3])});
      }
    asm volatile("s_waitcnt lgkmcnt(0)" ::: "memory");
#pragma unroll
    for (int a = 0; a < 4; ++a) {
      const u32 row = 8 * a + (lane >> 3), nr = 32 * w + row;
      const u32x4 v = ldl<u32x4>(slot0 + a * 1024 + lane * 16);
      if (nr < NPB && n0 + nr < (u32)N) *(u32x4*)((char*)out + (size_t)(n0 + nr) * 128 + ((a & 1) ? dch1 : dch0)) = v;
    }
  } else {
    const int nn = min(NPB, N - n0);
    u32* po = (u32*)(out + (size_t)N * 64 + (size_t)n0 * 3);
    for (int i = lane; 2 * i < 3 * nn; i += 64) {
      const int e0 = 2 * i, e1 = e0 + 1, a0 = e0 / 3, a1 = e1 / 3, c0 = e0 - 3 * a0, c1 = e1 - 3 * a1;
      const float v0 = ldl<float>(l0 + L_PT + 16 * a0 + 4 * c0) + ldl<float>(l0 + L_PA + 16 * a0 + 4 * c0);
      const float v1 = ldl<float>(l0 + L_PT + 16 * a1 + 4 * c1) + ldl<float>(l0 + L_PA + 16 * a1 + 4 * c1);
      po[i] = cvtpk(v0, v1);
    }
  }
  STAMP(19);
  }
  }
  if (slow) slow_bucket(h, pos, ea, src, dst, we, wx, wh, out, N, E, bk, l0);
  STAMP_DUMP(160u << 20, bk * 8 + w);
}

__global__ void gen_zero(float* p, int n) { int i = blockIdx.x * blockDim.x + threadIdx.x; if (i < n) p[i] = 0.f; }
__global__ void gen_edge(const u16* h, const u16* pos, const u16* ea, const int* src, const int* dst, const u16* we, const u16* wx, float* mi, float* pd, int E) {
  const int lane = threadIdx.x & 63, w = threadIdx.x >> 6, nw = blockDim.x >> 6;
  const float wxf = bf2f(wx[lane]);
  for (int e = blockIdx.x * nw + w; e < E; e += gridDim.x * nw) {
    const int s = src[e], d = dst[e];
    float df[3], d2 = 0.f;
#pragma unroll
    for (int k = 0; k < 3; ++k) { df[k] = lo2f(cvtpk(bf2f(pos[(size_t)d * 3 + k]) - bf2f(pos[(size_t)s * 3 + k]), 0.f)); d2 += df[k] * df[k]; }
    d2 = lo2f(cvtpk(d2, 0.f));
    const float hd = bf2f(h[(size_t)d * 64 + lane]), hs = bf2f(h[(size_t)s * 64 + lane]);
    float z = 0.f;
#pragma unroll 4
    for (int k = 0; k < 64; ++k) z += __shfl(hd, k) * bf2f(we[k * 64 + lane]) + __shfl(hs, k) * bf2f(we[(64 + k) * 64 + lane]);
    z += d2 * bf2f(we[128 * 64 + lane]) + bf2f(ea[e]) * bf2f(we[129 * 64 + lane]);
    const float m = lo2f(cvtpk(silu_f(z), 0.f));
    float cs = m * wxf;
#pragma unroll
    for (int o = 32; o > 0; o >>= 1) cs += __shfl_xor(cs, o);
    atomicAdd(&mi[(size_t)d * 64 + lane], m);
    if (lane < 3) atomicAdd(&pd[(size_t)d * 3 + lane], (lane == 0 ? df[0] : lane == 1 ? df[1] : df[2]) * cs);
  }
}
__global__ void gen_node(const u16* h, const u16* pos, const u16* wh, const float* mi, const float* pd, u16* out, int N) {
  const int lane = threadIdx.x & 63, w = threadIdx.x >> 6, nw = blockDim.x >> 6;
  for (int n = blockIdx.x * nw + w; n < N; n += gridDim.x * nw) {
    const float hv = bf2f(h[(size_t)n * 64 + lane]), mv = lo2f(cvtpk(mi[(size_t)n * 64 + lane], 0.f));
    float z = 0.f;
#pragma unroll 4
    for (int k = 0; k < 64; ++k) z += __shfl(hv, k) * bf2f(wh[k * 64 + lane]) + __shfl(mv, k) * bf2f(wh[(64 + k) * 64 + lane]);
    out[(size_t)n * 64 + lane] = (u16)cvtpk(hv + silu_f(z), 0.f);
    if (lane < 3) out[(size_t)N * 64 + (size_t)n * 3 + lane] = (u16)cvtpk(bf2f(pos[(size_t)n * 3 + lane]) + pd[(size_t)n * 3 + lane], 0.f);
  }
}

extern "C" void kernel_launch(void* const* d_in, const int* in_sizes, int n_in, void* d_out, int out_size, void* d_ws, size_t ws_size, hipStream_t stream) {
  const u16* h = (const u16*)d_in[0];
  const u16* pos = (const u16*)d_in[1];
  const u16* ea = (const u16*)d_in[2];
  const int* src = (const int*)d_in[3];
  const int* dst = (const int*)d_in[4];
  const u16* we = (const u16*)d_in[5];
  const u16* wx = (const u16*)d_in[6];
  const u16* wh = (const u16*)d_in[7];
  const int N = in_sizes[0] / 64, E = in_sizes[3];
  const int CS = ((E + NCH - 1) / NCH + 3) & ~3;
  const bool fast = N >= 2 && N <= NPB * NBK && (N * 6) % 64 != 0 && (E & 3) == 0 && E > 0 && CS <= 4096 && W_REC + (size_t)NCH * CS * 8 <= ws_size;
  if (fast) {
    (void)hipFuncSetAttribute((const void*)egnn_fused, hipFuncAttributeMaxDynamicSharedMemorySize, L_END);
    egnn_fused<<<NBK, 512, L_END, stream>>>(h, pos, ea, src, dst, we, wx, wh, (char*)d_ws, (u16*)d_out, N, E, CS);
  } else {
    float* mi = (float*)d_ws; float* pd = mi + (size_t)N * 64;
    const int nz = N * 67;
    gen_zero<<<(nz + 255) / 256, 256, 0, stream>>>(mi, nz);
    gen_edge<<<2048, 256, 0, stream>>>(h, pos, ea, src, dst, we, wx, mi, pd, E);
    gen_node<<<1024, 256, 0, stream>>>(h, pos, wh, mi, pd, (u16*)d_out, N);
  }
}
```

```cpp
#include <hip/hip_runtime.h>
#include <hip/hip_bf16.h>
#include <stdint.h>

typedef unsigned short u16;
typedef unsigned u32;
typedef short bf16x8 __attribute__((ext_vector_type(8)));
typedef short s16x4 __attribute__((ext_vector_type(4)));
typedef float f32x16 __attribute__((ext_vector_type(16)));
typedef float f32x4 __attribute__((ext_vector_type(4)));
typedef unsigned u32x4 __attribute__((ext_vector_type(4)));
typedef unsigned u32x2 __attribute__((ext_vector_type(2)));
#define LAS __attribute__((address_space(3)))

#define POLLEARLY 0
#define HBPOS 2
constexpr int NPB = 196;
constexpr int NBK = 256;
constexpr int NCH = 256;
constexpr int MAXT = 112;
constexpr float LOG2E = 1.4426950408889634f, LN2 = 0.6931471805599453f;

constexpr int NT1 = MAXT + 1;
constexpr int RS = 144;
constexpr int XW = 14;
constexpr int L_STG = 0;
constexpr int L_R = 65536;
constexpr int L_HM = L_R + NT1 * 512;
constexpr int L_PT = L_HM + 6 * 32 * RS + 1024;
constexpr int L_PA = L_PT + NPB * 16;
constexpr int L_CNT = L_PA + NPB * 16;
constexpr int L_WX = L_CNT + 1024;
constexpr int L_WR = L_WX + 128;
constexpr int L_OH = L_WR + 256;
constexpr int L_Z = L_OH + 256;
constexpr int L_MISC = L_Z + 1024;
constexpr int L_END = L_MISC + 64;
constexpr int L_SREC = L_STG;
constexpr int L_WRAW = L_STG + 32768;
constexpr int L_HC = L_STG + 49152;
constexpr int L_HS = L_HC + 2048;
static_assert(L_HM % 16 == 0 && L_PT % 16 == 0 && L_CNT % 16 == 0 && L_Z % 16 == 0 && L_WX % 16 == 0 && L_OH % 16 == 0, "align");
static_assert(L_END <= 160 * 1024, "lds");

constexpr size_t W_EP = 0;
constexpr size_t W_TAB = 4096;
constexpr size_t W_REC = W_TAB + NCH * NBK * 16;

static __device__ __forceinline__ float bf2f(u32 v) { return __uint_as_float(v << 16); }
typedef __bf16 bf16x2_t __attribute__((ext_vector_type(2)));
typedef float f32x2 __attribute__((ext_vector_type(2)));
static __device__ __forceinline__ u32 cvtpk(float lo, float hi) { return __builtin_bit_cast(u32, __builtin_convertvector(f32x2{lo, hi}, bf16x2_t)); }
static __device__ __forceinline__ float lo2f(u32 v) { return __uint_as_float(v << 16); }
static __device__ __forceinline__ float hi2f(u32 v) { return __uint_as_float(v & 0xffff0000u); }
template <int CTRL, int RM = 0xf> static __device__ __forceinline__ u32 dpp0(u32 x) { return (u32)__builtin_amdgcn_update_dpp(0, (int)x, CTRL, RM, 0xf, true); }
static __device__ __forceinline__ u32 wave_incl_scan(u32 x) {
  x += dpp0<0x111>(x); x += dpp0<0x112>(x); x += dpp0<0x114>(x); x += dpp0<0x118>(x);
  x += dpp0<0x142, 0xa>(x); x += dpp0<0x143, 0xc>(x); return x; }
#define MFMA(a, b, c) __builtin_amdgcn_mfma_f32_32x32x16_bf16(a, b, c, 0, 0, 0)
#define SB() __builtin_amdgcn_sched_barrier(0)
#define LBAR() asm volatile("s_waitcnt lgkmcnt(0)\n\ts_barrier" ::: "memory")
template <class T> static __device__ __forceinline__ T ldl(u32 a) { return *(LAS const T*)(size_t)a; }
template <class T> static __device__ __forceinline__ void stl(u32 a, T v) { *(LAS T*)(size_t)a = v; }
static __device__ __forceinline__ s16x4 ldtr(u32 a) { typedef short v4 __attribute__((ext_vector_type(4)));
  return __builtin_bit_cast(s16x4, __builtin_amdgcn_ds_read_tr16_b64_v4i16((LAS v4*)(size_t)a)); }
static __device__ __forceinline__ float silu_f(float z) { return z * __builtin_amdgcn_rcpf(1.f + __builtin_amdgcn_exp2f(-LOG2E * z)); }

#define STAMP(i)
#define STAMP_DECL
#define STAMP_DUMP(base, idx)
struct Dma { const char* b0; const char* b1; const char* b2; const char* b3; };
static __device__ __forceinline__ void dma4(u32 v0, u32 v1, u32 v2, u32 v3, const Dma& d, u32 lds) {
  u32 sv;
  asm volatile("s_mov_b32 %0, m0\n\ts_mov_b32 m0, %9\n\ts_nop 0\n\t"
               "global_load_lds_dwordx4 %1, %5\n\t"
               "global_load_lds_dwordx4 %2, %6 offset:1024\n\t"
               "global_load_lds_dwordx4 %3, %7 offset:2048\n\t"
               "global_load_lds_dwordx4 %4, %8 offset:3072\n\t"
               "s_mov_b32 m0, %0"
               : "=&s"(sv) : "v"(v0), "v"(v1), "v"(v2), "v"(v3), "s"(d.b0), "s"(d.b1), "s"(d.b2), "s"(d.b3), "s"(lds) : "memory");
}

static __device__ __forceinline__ void slow_bucket(const u16* __restrict__ h, const u16* __restrict__ pos, const u16* __restrict__ ea, const int* __restrict__ src,
                                                const int* __restrict__ dst, const u16* __restrict__ we, const u16* __restrict__ wx,
                                                const u16* __restrict__ wh, u16* __restrict__ out, int N, int E, int bk, u32 l0) {
  const int tid = threadIdx.x, lane = tid & 63, w = tid >> 6, n0 = bk * NPB;
  LAS float* macc = (LAS float*)(size_t)(l0 + L_STG);
  LAS float* pacc = (LAS float*)(size_t)(l0 + L_PA);
  __syncthreads();
  for (int i = tid; i < NPB * 64; i += 512) macc[i] = 0.f;
  for (int i = tid; i < NPB * 4; i += 512) pacc[i] = 0.f;
  __syncthreads();
  const float wxf = bf2f(wx[lane]);
  for (int eb = 64 * w; eb < E; eb += 512) {
    const int dv = eb + lane < E ? dst[eb + lane] : -1;
    unsigned long long m = __builtin_amdgcn_ballot_w64((u32)(dv - n0) < (u32)NPB && dv >= 0);
    while (m) {
      const int l = __builtin_ctzll(m); m &= m - 1;
      const int e = eb + l, d = __builtin_amdgcn_readlane(dv, l), s = src[e], dl = d - n0;
      float df[3], d2 = 0.f;
#pragma unroll
      for (int k = 0; k < 3; ++k) { df[k] = lo2f(cvtpk(bf2f(pos[d * 3 + k]) - bf2f(pos[s * 3 + k]), 0.f)); d2 += df[k] * df[k]; }
      d2 = lo2f(cvtpk(d2, 0.f));
      const float hd = bf2f(h[(size_t)d * 64 + lane]), hs = bf2f(h[(size_t)s * 64 + lane]);
      float z = 0.f;
#pragma unroll 4
      for (int k = 0; k < 64; ++k) z += __shfl(hd, k) * bf2f(we[k * 64 + lane]) + __shfl(hs, k) * bf2f(we[(64 + k) * 64 + lane]);
      z += d2 * bf2f(we[128 * 64 + lane]) + bf2f(ea[e]) * bf2f(we[129 * 64 + lane]);
      const float mm = lo2f(cvtpk(silu_f(z), 0.f));
      float cs = mm * wxf;
#pragma unroll
      for (int o = 32; o > 0; o >>= 1) cs += __shfl_xor(cs, o);
      atomicAdd((float*)&macc[dl * 64 + lane], mm);
      if (lane < 3) atomicAdd((float*)&pacc[dl * 4 + lane], (lane == 0 ? df[0] : lane == 1 ? df[1] : df[2]) * cs);
    }
  }
  __syncthreads();
  for (int nl = w; nl < NPB; nl += 8) {
    const int n = n0 + nl; if (n >= N) break;
    const float hv = bf2f(h[(size_t)n * 64 + lane]), mv = lo2f(cvtpk(macc[nl * 64 + lane], 0.f));
    float z = 0.f;
#pragma unroll 4
    for (int k = 0; k < 64; ++k) z += __shfl(hv, k) * bf2f(wh[k * 64 + lane]) + __shfl(mv, k) * bf2f(wh[(64 + k) * 64 + lane]);
    out[(size_t)n * 64 + lane] = (u16)cvtpk(hv + silu_f(z), 0.f);
    if (lane < 3) out[(size_t)N * 64 + (size_t)n * 3 + lane] = (u16)cvtpk(bf2f(pos[n * 3 + lane]) + pacc[nl * 4 + lane], 0.f);
  }
}
static __device__ __forceinline__ u32 umul24(u32 a, u32 b) { return (u32)__umul24(a, b); }
static __device__ __forceinline__ u32 mad16lo(u32 a, u32 b, u32 c) { u32 r; asm("v_mad_u32_u16 %0, %1, %2, %3" : "=v"(r) : "v"(a), "s"(b), "v"(c)); return r; }
static __device__ __forceinline__ u32 mad16hi(u32 a, u32 b, u32 c) { u32 r; asm("v_mad_u32_u16 %0, %1, %2, %3 op_sel:[1,0,0,0]" : "=v"(r) : "v"(a), "s"(b), "v"(c)); return r; }
static __device__ __forceinline__ bf16x8 cat8(s16x4 a, s16x4 b) { return __builtin_shufflevector(a, b, 0, 1, 2, 3, 4, 5, 6, 7); }
extern "C" __device__ float llvm_fmul_legacy(float, float) __asm("llvm.amdgcn.fmul.legacy");
static __device__ __forceinline__ float vmul(float a, float b) { return llvm_fmul_legacy(a, b); }
static __device__ __forceinline__ void wait_dma(u32 a, u32 b) {
  asm volatile("s_cmp_lt_u32 %0, %1\n\ts_cbranch_scc1 1f\n\ts_waitcnt vmcnt(0)\n1:\n\ts_waitcnt vmcnt(4)" :: "s"(a), "s"(b) : "scc", "memory"); }
static __device__ __forceinline__ void dma4_if(u32 a, u32 b, u32 v0, u32 v1, u32 v2, u32 v3, const Dma& d, u32 lds) {
  u32 sv;
  asm volatile("s_cmp_lt_u32 %10, %11\n\ts_cbranch_scc0 1f\n\t"
               "s_mov_b32 %0, m0\n\ts_mov_b32 m0, %9\n\ts_nop 0\n\t"
               "global_load_lds_dwordx4 %1, %5\n\t"
               "global_load_lds_dwordx4 %2, %6 offset:1024\n\t"
               "global_load_lds_dwordx4 %3, %7 offset:2048\n\t"
               "global_load_lds_dwordx4 %4, %8 offset:3072\n\t"
               "s_mov_b32 m0, %0\n1:"
               : "=&s"(sv) : "v"(v0), "v"(v1), "v"(v2), "v"(v3), "s"(d.b0), "s"(d.b1), "s"(d.b2), "s"(d.b3), "s"(lds), "s"(a), "s"(b) : "scc", "memory");
}
template <int V> struct IC { static constexpr int value = V; };

constexpr u32 SPIN_MAX = 1u << 15;
typedef __attribute__((address_space(1))) unsigned long long gu64;

__global__ void __launch_bounds__(512, 2) egnn_fused(const u16* __restrict__ h, const u16* __restrict__ pos, const u16* __restrict__ ea, const int* __restrict__ src,
                                                     const int* __restrict__ dst, const u16* __restrict__ we, const u16* __restrict__ wx, const u16* __restrict__ wh,
                                                     char* __restrict__ ws, u16* __restrict__ out, int N, int E, int CS) {
  extern __shared__ __attribute__((aligned(16))) char smem[];
  const u32 l0 = (u32)(size_t)(LAS char*)smem;
  const int tid = threadIdx.x, lane = tid & 63;
  const int w = __builtin_amdgcn_readfirstlane(tid >> 6);
  const int c = blockIdx.x;
  const int bk = (blockIdx.x & 7) * 32 + (blockIdx.x >> 3);
  const int n0 = bk * NPB;
  STAMP_DECL STAMP(0);

  const int grp = lane >> 4, j16 = lane & 15, e32 = lane & 31, hh = lane >> 5;
  const int e0 = c * CS + 4 * tid;
  const bool act0 = 4 * tid < CS && e0 < E, act1 = 4 * tid + 2048 < CS && e0 + 2048 < E;
  const __amdgpu_buffer_rsrc_t rdst = __builtin_amdgcn_make_buffer_rsrc((void*)dst, 0, E * 4, 0x00020000), rsrc_ = __builtin_amdgcn_make_buffer_rsrc((void*)src, 0, E * 4, 0x00020000);
  const __amdgpu_buffer_rsrc_t rea = __builtin_amdgcn_make_buffer_rsrc((void*)ea, 0, E * 2, 0x00020000);
  const u32 eo0 = act0 ? (u32)e0 : 0x1fffff00u, eo1 = act1 ? (u32)e0 + 2048 : 0x1fffff00u;
  u32x4 d4[2], s4[2]; u32x2 a4[2];
  d4[0] = __builtin_bit_cast(u32x4, __builtin_amdgcn_raw_buffer_load_b128(rdst, eo0 * 4, 0, 0));
  d4[1] = __builtin_bit_cast(u32x4, __builtin_amdgcn_raw_buffer_load_b128(rdst, eo1 * 4, 0, 0));
  s4[0] = __builtin_bit_cast(u32x4, __builtin_amdgcn_raw_buffer_load_b128(rsrc_, eo0 * 4, 0, 0));
  s4[1] = __builtin_bit_cast(u32x4, __builtin_amdgcn_raw_buffer_load_b128(rsrc_, eo1 * 4, 0, 0));
  a4[0] = __builtin_bit_cast(u32x2, __builtin_amdgcn_raw_buffer_load_b64(rea, eo0 * 2, 0, 0));
  a4[1] = __builtin_bit_cast(u32x2, __builtin_amdgcn_raw_buffer_load_b64(rea, eo1 * 2, 0, 0));
  const unsigned long long epv = __hip_atomic_load((gu64*)(ws + W_EP), __ATOMIC_RELAXED, __HIP_MEMORY_SCOPE_AGENT);
  u32x4 wt[2];
#pragma unroll
  for (int i = 0; i < 2; ++i) wt[i] = ((const u32x4*)we)[tid + 512 * i];
  const u32 wxr = wx[16 * ((tid >> 4) & 3) + (tid & 3) + 8 * ((tid >> 2) & 1) + 4 * ((tid >> 3) & 1)];
  const u32 wrr = we[128 * 64 + (tid & 127)];
  const int nmax = N - 1;
  const __amdgpu_buffer_rsrc_t rpos = __builtin_amdgcn_make_buffer_rsrc((void*)pos, 0, N * 6 + 2, 0x00020000);
  const u32x2 ptile = __builtin_bit_cast(u32x2, __builtin_amdgcn_raw_buffer_load_b64(rpos, min(n0 + tid, nmax) * 6, 0, 0));
  const Dma dh = {(const char*)h, (const char*)h - 1024, (const char*)h - 2048, (const char*)h - 3072};
  const u32 dch0 = 16 * ((lane & 7) ^ (lane >> 4)), dch1 = dch0 ^ 64;
  const u32 hbs = l0 + L_HM + 32 * RS * w;
  auto dma_rows = [&](u32 slot) {
    const u32 rb = (u32)(n0 + 32 * w) + (lane >> 3), nm = (u32)nmax;
    dma4(min(rb, nm) * 128 + dch0, min(rb + 8, nm) * 128 + dch1, min(rb + 16, nm) * 128 + dch0, min(rb + 24, nm) * 128 + dch1, dh, slot); };
  auto dma_hb = [&]() {
    if (w < 6) dma_rows(hbs);
    else if (w == 6) { u32 sv; const u32 vo = min((u32)(n0 + 192) + (lane >> 3), (u32)nmax) * 128 + dch0;
      asm volatile("s_mov_b32 %0, m0\n\ts_mov_b32 m0, %3\n\ts_nop 0\n\tglobal_load_lds_dwordx4 %1, %2\n\ts_mov_b32 m0, %0" : "=&s"(sv) : "v"(vo), "s"(h), "s"(hbs) : "memory"); } };
  dma_hb();
  if (tid < 256) { stl<u32>(l0 + L_CNT + 4 * tid, 0u); stl<u32x2>(l0 + L_HC + 8 * tid, u32x2{0, 0}); }
  if (tid < 16) stl<u32>(l0 + L_MISC + 4 * tid, tid < 8 ? 0xffffffffu : 0u);
  if (tid < 256) stl<u32>(l0 + L_Z + 4 * tid, 0u);
  const u32 lst = l0 + L_R + 2048 * w;
  stl<u32x4>(lst + 32 * lane, u32x4{8, 8, 8, 8}); stl<u32x4>(lst + 32 * lane + 16, u32x4{8, 8, 8, 8});
  if (tid < NPB) stl<f32x4>(l0 + L_PA + 16 * tid, f32x4{0, 0, 0, 0});
  if (tid < 64) { const int u = tid < 36 ? tid : tid - 36, e = u >> 2, lim = tid < 36 ? 8 : 6;
    stl<u32>(l0 + L_OH + 4 * tid, (e < lim && (e >> 1) == (u & 3)) ? ((e & 1) ? 0x3f800000u : 0x3f80u) : 0u); }
  STAMP(22);
  LBAR();
  STAMP(1);
  STAMP(2);
  const u32 tag = __builtin_amdgcn_readfirstlane((u32)epv) + 1u, ephi = __builtin_amdgcn_readfirstlane((u32)(epv >> 32)), tag6 = ephi & 63u;
  const u32 tagx = (tag >> 16 ^ tag6) & 63u, tagy = tag & 0xffffu;
  u32 b[8], r[8]; u32x2 rc[8];
#pragma unroll
  for (int j = 0; j < 8; ++j) {
    b[j] = umul24(d4[j >> 2][j & 3], 85599u) >> 24;
    const u32 eav = (j & 1) ? (a4[j >> 2][(j >> 1) & 1] & 0xffff0000u) : (a4[j >> 2][(j >> 1) & 1] << 16);
    rc[j] = u32x2{s4[j >> 2][j & 3] | (d4[j >> 2][j & 3] - umul24(b[j], NPB)) << 18 | tagx << 26, eav | tagy};
  }
#define HIST(j) r[j] = (u32)__hip_atomic_fetch_add((LAS unsigned long long*)(size_t)(l0 + L_HC + 8 * b[j]), (unsigned long long)(rc[j].x + rc[j].y) << 32 | 1u, __ATOMIC_RELAXED, __HIP_MEMORY_SCOPE_WORKGROUP);
  if (act0) {
#pragma unroll
    for (int j = 0; j < 4; ++j) HIST(j)
  }
  if (act1) {
#pragma unroll
    for (int j = 4; j < 8; ++j) HIST(j)
  }
#pragma unroll
  for (int i = 0; i < 2; ++i) {
    const u32 q = tid + 512 * i;
    u32x4 o;
#pragma unroll
    for (int k = 0; k < 4; ++k) o[k] = cvtpk(lo2f(wt[i][k]) * -LOG2E, hi2f(wt[i][k]) * -LOG2E);
    stl<u32x4>(l0 + L_WRAW + ((q * 16) ^ ((q >> 4 & 1) << 6)), o);
  }
  if (tid < 64) stl<u16>(l0 + L_WX + 2 * tid, (u16)wxr);
  if (tid < 128) stl<u16>(l0 + L_WR + 2 * tid, (u16)cvtpk(bf2f(wrr) * -LOG2E, 0.f));
  if (tid < NPB) stl<f32x4>(l0 + L_PT + 16 * tid, f32x4{lo2f(ptile.x), hi2f(ptile.x), lo2f(ptile.y), 0.f});
  asm volatile("" :: "v"(s4[0]), "v"(s4[1]), "v"(a4[0]), "v"(a4[1]), "v"(ptile), "v"(epv));
  LBAR();
  STAMP(3);
  {
    const u32x4 c0 = ldl<u32x4>(l0 + L_HC + 32 * lane), c1 = ldl<u32x4>(l0 + L_HC + 32 * lane + 16);
    const u32x4 cv = {c0.x, c0.z, c1.x, c1.z};
    const u32 s1 = cv.x + cv.y, s2 = s1 + cv.z, s3 = s2 + cv.w;
    const u32 ex = wave_incl_scan(s3) - s3;
    const u32x4 st = {ex, ex + cv.x, ex + s1, ex + s2};
    stl<u32x4>(l0 + L_HS + 16 * lane, st);
    if (w < 4) {
      const __amdgpu_buffer_rsrc_t rtab = __builtin_amdgcn_make_buffer_rsrc((void*)(ws + W_TAB), 0, NCH * NBK * 16, 0x00020000);
      const u32 tv = w == 0 ? st.x | cv.x << 13 : w == 1 ? st.y | cv.y << 13 : w == 2 ? st.z | cv.z << 13 : st.w | cv.w << 13;
      const u32 cs = w == 0 ? c0.y : w == 1 ? c0.w : w == 2 ? c1.y : c1.w;
      __builtin_amdgcn_raw_buffer_store_b128(u32x4{tv | tag6 << 26, tag, cs, tag}, rtab, ((4 * lane + w) * NCH + c) * 16, 0, 16);
    }
  }
  {
    u32 sp[8];
#pragma unroll
    for (int j = 0; j < 8; ++j) sp[j] = ldl<u32>(l0 + L_HS + 4 * b[j]);
#pragma unroll
    for (int j = 0; j < 8; ++j) if (j < 4 ? act0 : act1) stl<u32x2>(l0 + L_SREC + 8 * (sp[j] + r[j]), rc[j]);
  }
  LBAR();
  STAMP(4);
  const __amdgpu_buffer_rsrc_t rrec = __builtin_amdgcn_make_buffer_rsrc((void*)(ws + W_REC), 0, NCH * CS * 8, 0x00020000);
  {
    const int n = min(CS, E - c * CS);
    u32x4 cp[4];
#pragma unroll
    for (int i = 0; i < 4; ++i) cp[i] = ldl<u32x4>(l0 + L_SREC + 16 * (tid + 512 * i));
#pragma unroll
    for (int i = 0; i < 4; ++i) { const int q = tid + 512 * i;
      __builtin_amdgcn_raw_buffer_store_b128(cp[i], rrec, 2 * q < n ? (c * CS + 2 * q) * 8 : 0x7ffffff0, 0, 16); }
  }
  STAMP(5);
  const u32 r4 = (lane >> 2) & 3, cofs = 32 * ((lane >> 4) & 1) + 8 * (lane & 3);
  const u32 wtr = (8 * hh + r4) * 128 + cofs;
  bf16x8 W[8], W1[8];
  {
    const u32 wa0_ = l0 + L_WRAW + wtr + 64 * (r4 >> 1), wa1_ = l0 + L_WRAW + wtr + 64 - 64 * (r4 >> 1);
#pragma unroll
    for (int i = 0; i < 8; ++i) { const u32 a = ((i & 1) ? wa1_ : wa0_) + (i >> 1) * 2048; W1[i] = cat8(ldtr(a), ldtr(a + 512)); W[i] = cat8(ldtr(a + 8192), ldtr(a + 8192 + 512)); }
  }
  STAMP(6);
  if (n0 >= N) { asm volatile("s_waitcnt vmcnt(0)" ::: "memory"); return; }
  u32 ent, cks;
  {
    const char* tb = ws + W_TAB; const u32 vo = (bk * NCH + 32 * w + e32) * 16;
    u32x4 g; bool ok; u32 spins = 0;
    do {
      asm volatile("global_load_dwordx4 %0, %1, %2 sc1\n\ts_waitcnt vmcnt(0)" : "=&v"(g) : "v"(vo), "s"(tb) : "memory");
      ok = __builtin_amdgcn_ballot_w64(g.y != tag || g.w != tag || (g.x >> 26) != tag6) == 0;
      if (!ok) __builtin_amdgcn_s_sleep(1);
    } while (!ok && ++spins < SPIN_MAX);
    ent = ok && lane < 32 ? g.x : 0u; cks = lane < 32 ? 0u - g.z : 0u;
    if (!ok) stl<u32>(l0 + L_MISC + 36, 1u);
  }
  STAMP(7);
  u32 Tw, adr[8];
  {
    const u32 cn = (ent >> 13) & 0x1fff;
    u32 inc = cn; inc += dpp0<0x111>(inc); inc += dpp0<0x112>(inc); inc += dpp0<0x114>(inc); inc += dpp0<0x118>(inc); inc += dpp0<0x142, 0xa>(inc);
    Tw = __builtin_amdgcn_readlane(inc, 31);
    const u32 pre = inc - cn;
    const u32 delta = 8 * ((u32)(32 * w + lane) * (u32)CS + (ent & 0x1fff) - pre);
    const u32 dprev = (u32)__builtin_amdgcn_update_dpp(8, (int)delta, 0x138, 0xf, 0xf, false);
    if (lane < 32 && pre < 512) __hip_atomic_fetch_add((LAS u32*)(size_t)(lst + 4 * pre), delta - dprev, __ATOMIC_RELAXED, __HIP_MEMORY_SCOPE_WORKGROUP);
    const u32x4 m0 = ldl<u32x4>(lst + 32 * lane), m1 = ldl<u32x4>(lst + 32 * lane + 16);
    u32 a[8];
    a[0] = m0.x; a[1] = a[0] + m0.y; a[2] = a[1] + m0.z; a[3] = a[2] + m0.w; a[4] = a[3] + m1.x; a[5] = a[4] + m1.y; a[6] = a[5] + m1.z; a[7] = a[6] + m1.w;
    const u32 ex = wave_incl_scan(a[7]) - a[7];
    stl<u32x4>(lst + 32 * lane, u32x4{a[0] + ex, a[1] + ex, a[2] + ex, a[3] + ex});
    stl<u32x4>(lst + 32 * lane + 16, u32x4{a[4] + ex, a[5] + ex, a[6] + ex, a[7] + ex});
#pragma unroll
    for (int s = 0; s < 8; ++s) adr[s] = ldl<u32>(lst + 256 * s + 4 * lane);
  }
  if (Tw > 512) { stl<u32>(l0 + L_MISC + 36, 1u); Tw = 0; }
  const bool big = Tw > 448;
#define SLOTS(...) { _Pragma("unroll") for (int s = 0; s < 7; ++s) { __VA_ARGS__ } if (big) { constexpr int s = 7; __VA_ARGS__ } }
#define V(s) ((u32)(64 * (s) + lane) < Tw)
  u32x2 rec[8];
#define LDREC(off) __builtin_bit_cast(u32x2, __builtin_amdgcn_raw_buffer_load_b64(rrec, off, 0, 16))
#pragma unroll
  for (int s = 0; s < 8; ++s) { adr[s] = V(s) ? adr[s] : 0x7ffffff0u; rec[s] = LDREC(adr[s]); }
  const u32 swz = (e32 >> 1) & 7;
  if (w < 7) {
    f32x16 A0, A1;
#pragma unroll
    for (int q = 0; q < 16; ++q) { A0[q] = 0.f; A1[q] = 0.f; }
#pragma unroll
    for (int s = 0; s < 4; ++s) { const bf16x8 hb = ldl<bf16x8>(hbs + e32 * 128 + 16 * ((2 * s + hh) ^ swz)); A0 = MFMA(W1[2 * s], hb, A0); A1 = MFMA(W1[2 * s + 1], hb, A1); }
    const u32 nl = 32 * w + e32;
    if (nl <= NPB) {
      const u32 a = l0 + L_HM + nl * RS + 8 * hh;
#pragma unroll
      for (int q = 0; q < 4; ++q) {
        stl<u32x2>(a + 16 * q, u32x2{cvtpk(A0[4 * q], A0[4 * q + 1]), cvtpk(A0[4 * q + 2], A0[4 * q + 3])});
        stl<u32x2>(a + 64 + 16 * q, u32x2{cvtpk(A1[4 * q], A1[4 * q + 1]), cvtpk(A1[4 * q + 2], A1[4 * q + 3])});
      }
    }
  }
  __builtin_amdgcn_sched_barrier(0);
  STAMP(8);
  {
    auto missing = [&]() {
      u32 acc = cks;
#pragma unroll
      for (int s = 0; s < 8; ++s) acc += rec[s].x + rec[s].y;
      return __builtin_amdgcn_readlane(wave_incl_scan(acc), 63) != 0; };
    if (Tw != 0 && missing()) {
      u32 spins = 0; bool miss;
      do {
        asm volatile("s_sleep 1" ::: "memory");
#pragma unroll
        for (int s = 0; s < 8; ++s) rec[s] = LDREC(adr[s]);
        miss = missing();
      } while (miss && ++spins < SPIN_MAX);
      if (miss) { stl<u32>(l0 + L_MISC + 36, 1u); Tw = 0; }
    }
  }
#pragma unroll
  for (int s = 0; s < 8; ++s) asm volatile("" : "+v"(rec[s].x), "+v"(rec[s].y));
  STAMP(9);
  const u32 tg = tagx << 10;
  u32 rank[8]; u32x2 ps[8];
  const u32 cntb = l0 + L_CNT - tg, six = 6;
#define GATHER(s) { __builtin_amdgcn_sched_barrier(0); ps[s] = __builtin_bit_cast(u32x2, __builtin_amdgcn_raw_buffer_load_b64(rpos, mad16lo(rec[s].x, six, 0), 0, 0)); __builtin_amdgcn_sched_barrier(0); }
  GATHER(0)
  SLOTS(if (V(s)) rank[s] = __hip_atomic_fetch_add((LAS u32*)(size_t)(cntb + (rec[s].x >> 16)), 1u, __ATOMIC_RELAXED, __HIP_MEMORY_SCOPE_WORKGROUP);)
  GATHER(1)
  STAMP(10);
  LBAR();
  STAMP(11);
  if (blockIdx.x == 0 && tid == 0)
    __hip_atomic_store((gu64*)(ws + W_EP), (epv & 0xffffffff00000000ull) | tag, __ATOMIC_RELAXED, __HIP_MEMORY_SCOPE_AGENT);
  u32 wa0, wa1, wtb, wnt, wend; bool slow;
  {
    const u32x4 cv = ldl<u32x4>(l0 + L_CNT + 16 * lane);
    const u32 flag = ldl<u32>(l0 + L_MISC + 36);
    const u32 s1 = cv.x + cv.y, s2 = s1 + cv.z, s3 = s2 + cv.w;
    const u32 inc = wave_incl_scan(s3), ex = inc - s3;
    const u32 T = __builtin_amdgcn_readlane(inc, 63);
    const u32 off[4] = {ex, ex + cv.x, ex + s1, ex + s2};
    GATHER(2)
    const float rT = T ? 8.f / (float)T : 0.f;
    u32 wv[4];
#pragma unroll
    for (int i = 0; i < 4; ++i) wv[i] = min(7u, (u32)((float)off[i] * rT));
    u32 prev = (u32)__builtin_amdgcn_update_dpp(-1, (int)wv[3], 0x138, 0xf, 0xf, false);
#pragma unroll
    for (int i = 0; i < 4; ++i) { const u32 n = 4 * lane + i;
      if (n < NPB && wv[i] != prev) stl<u32>(l0 + L_MISC + 4 * wv[i], off[i] << 8 | n);
      prev = wv[i]; }
    u32 cut = min(ldl<u32>(l0 + L_MISC + 4 * (lane & 7)), T << 8 | NPB);
    if (j16 >= 8) cut = T << 8 | NPB;
    GATHER(3)
    cut = min(cut, (u32)__builtin_amdgcn_update_dpp(-1, (int)cut, 0x101, 0xf, 0xf, false));
    cut = min(cut, (u32)__builtin_amdgcn_update_dpp(-1, (int)cut, 0x102, 0xf, 0xf, false));
    cut = min(cut, (u32)__builtin_amdgcn_update_dpp(-1, (int)cut, 0x104, 0xf, 0xf, false));
    const u32 nxt = (u32)__builtin_amdgcn_update_dpp(-1, (int)cut, 0x101, 0xf, 0xf, false);
    u32 len = j16 < 8 ? (nxt >> 8) - (cut >> 8) : 0, nt = (len + 31) >> 5;
    u32 tb = nt; tb += dpp0<0x111>(tb); tb += dpp0<0x112>(tb); tb += dpp0<0x114>(tb);
    const u32 ttot = __builtin_amdgcn_readlane(tb, 7);
    tb -= nt;
    const u32 delta = 32 * tb - (cut >> 8);
#pragma unroll
    for (int i = 0; i < 4; ++i) { const u32 o = off[i] + (u32)__builtin_amdgcn_ds_bpermute(4 * wv[i], (int)delta);
      if (lane < NPB / 4) stl<u32>(l0 + L_PT + 64 * lane + 16 * i + 12, o); }
    GATHER(4)
    wa0 = __builtin_amdgcn_readlane(cut, w) & 0xff; wa1 = __builtin_amdgcn_readlane(nxt, w) & 0xff;
    wtb = __builtin_amdgcn_readlane(tb, w); wnt = __builtin_amdgcn_readlane(nt, w);
    wend = 32 * wtb + __builtin_amdgcn_readlane(len, w);
    slow = ttot > MAXT || __builtin_amdgcn_readfirstlane(flag) != 0;
  }
  if (!slow) {
  STAMP(12);
  u32 pp[8]; f32x4 pd[8];
  const u32 ptb = l0 + L_PT - 4 * tg, four = 4;
  SLOTS(pd[s] = ldl<f32x4>(mad16hi(rec[s].x, four, ptb));)
  GATHER(5)
  SLOTS(pp[s] = l0 + L_R + 16 * (__float_as_uint(pd[s].w) + rank[s]); if (V(s)) stl<u32>(pp[s] + 12, rec[s].x);)
  GATHER(6)
  if (big) GATHER(7)
  {
    const u32 p = wend + lane;
    if (p < 32 * (wtb + wnt)) stl<u32x4>(l0 + L_R + 16 * p, u32x4{0, 0, 0, (4 * NPB + tg) << 16});
  }
  LBAR();
  STAMP(13);
  const u32 slot0 = l0 + L_STG + w * 8192;
  const u32 sraddr = l0 + L_R + 512 * wtb + 16 * (lane >> 3) + 12;
  const u32 c128 = 128;
  auto ldsrc = [&](u32 a) { return u32x4{ldl<u32>(a), ldl<u32>(a + 128), ldl<u32>(a + 256), ldl<u32>(a + 384)}; };
  auto issue = [&](u32 a, u32 b, u32x4 sv, u32 slot) {
    dma4_if(a, b, mad16lo(sv.x, c128, dch0), mad16lo(sv.y, c128, dch1), mad16lo(sv.z, c128, dch0), mad16lo(sv.w, c128, dch1), dh, slot);
  };
  SLOTS(asm volatile("" : "+v"(ps[s].x), "+v"(ps[s].y));)
  issue(0, wnt, ldsrc(sraddr), slot0);
  issue(1, wnt, ldsrc(sraddr + 512), slot0 + 4096);
  SLOTS(const float dx = pd[s].x - lo2f(ps[s].x); const float dy = pd[s].y - hi2f(ps[s].x); const float dz = pd[s].z - lo2f(ps[s].y);
        const float d2 = dx * dx + dy * dy + dz * dz;
        const u32 de_ = __builtin_amdgcn_perm(rec[s].y, cvtpk(d2, 0.f), 0x07060100u);
        if (V(s)) { stl<u32x2>(pp[s], u32x2{cvtpk(1.f, dx), cvtpk(dy, dz)}); stl<u32>(pp[s] + 8, de_); })
  STAMP(14);
  LBAR();
  STAMP(15);

  u32 sa[4];
#pragma unroll
  for (int s = 0; s < 4; ++s) sa[s] = slot0 + e32 * 128 + 16 * ((2 * s + hh) ^ swz);
  const u32 eq = 4 * hh + ((lane >> 2) & 3), slotA = 4 * ((lane >> 4) & 1) + (lane & 3);
  u32 zv = l0 + L_Z; asm volatile("" : "+v"(zv));
  u32 a_de = l0 + L_R + 512 * wtb + 16 * e32 + 8;
  u32 a_v = l0 + L_R + 512 * wtb + 16 * eq;
  u32 a_sr = sraddr + 1536;
  const u32 ohb = l0 + L_OH + (hh ? 144 : 0), ohl = hh ? 24 : 32, nh8 = (u32)(-32 * hh), dem = hh ? 0xffffffffu : 0u;
  const u32 pA = l0 + L_HM + (8 * hh + r4) * RS + cofs - (RS / 4) * tg;
  const bool spc = hh && r4 >= 2;
  const u32 pB = spc ? l0 + L_WR + (r4 - 2) * 128 + cofs : pA + 4 * RS;
  u32 pM = spc ? 0 : 1; asm volatile("" : "+v"(pM));
  f32x16 Y0, Y1, X0, X1, ZERO;
#pragma unroll
  for (int r = 0; r < 16; ++r) { Y0[r] = 0.f; Y1[r] = 0.f; ZERO[r] = 0.f; }
  u32 base = wa0, tgt = 4 * (wa0 + slotA) + tg;
  int bad = 0;
  const u32 fn = (lane & 31) >> 2, fi = lane & 3;
  u32 f_wx = l0 + L_WX + 16 * hh, f_m = l0 + L_HM + fn * RS + 8 * hh, f_p = l0 + L_PA + 16 * fn + 4 * (fi - 1);
  asm volatile("" : "+v"(f_wx), "+v"(f_m), "+v"(f_p));
  auto flush = [&]() {
    u32x4 c[4];
#pragma unroll
    for (int i = 0; i < 4; ++i) { c[0][i] = cvtpk(Y0[2 * i], Y0[2 * i + 1]); c[1][i] = cvtpk(Y0[8 + 2 * i], Y0[9 + 2 * i]);
                                  c[2][i] = cvtpk(Y1[2 * i], Y1[2 * i + 1]); c[3][i] = cvtpk(Y1[8 + 2 * i], Y1[9 + 2 * i]); }
    f32x16 D = ZERO;
#pragma unroll
    for (int i = 0; i < 4; ++i) D = MFMA(ldl<bf16x8>(f_wx + 32 * i), __builtin_bit_cast(bf16x8, c[i]), D);
    const u32 node = base + fn;
    if (node < wa1) {
      if (fi == 0) {
        const u32 a = f_m + base * RS;
#pragma unroll
        for (int q = 0; q < 4; ++q) {
          stl<u32x2>(a + 16 * q, u32x2{c[q >> 1][2 * (q & 1)], c[q >> 1][2 * (q & 1) + 1]});
          stl<u32x2>(a + 64 + 16 * q, u32x2{c[2 + (q >> 1)][2 * (q & 1)], c[2 + (q >> 1)][2 * (q & 1) + 1]});
        }
      } else if (hh == 0) stl<float>(f_p + 16 * base, D[0] * -LN2);
    }
  };
  if (wnt > 0) {
    bf16x8 As[4], Pb0, Pb1; u32x4 OH; u32x4 P0[2], P1[2];
    u32 lastc, lastn, aoh, aA, aB;
    u32 lastreal;
    lastreal = __builtin_amdgcn_readfirstlane(ldl<u32>(l0 + L_R + 16 * (wend - 1) + 12) >> 16);
    float tt[4];
    auto front = [&](u32 dsb, u32 valid) {
      const u32 first = __builtin_amdgcn_readlane(dsb, 0); lastn = min((u32)__builtin_amdgcn_readlane(dsb, 31), lastreal);
      const u32 wb = min(first, (u32)(4 * (NPB - XW)) + tg);
      bad = max(bad, (int)((lastn - wb) & valid));
      aoh = ohb + 4 * min(dsb - wb + nh8, ohl);
      const u32 wo = wb * (RS / 4);
      aA = pA + wo; aB = umul24(pM, wo) + pB;
    };
    auto rdwin = [&]() { OH = ldl<u32x4>(aoh);
      Pb0 = cat8(ldtr(aA), ldtr(aB)); Pb1 = cat8(ldtr(aA + 64), ldtr(aB + 64)); };
#define EA(X, b) { _Pragma("unroll") for (int k_ = 0; k_ < 4; ++k_) tt[k_] = __builtin_amdgcn_exp2f(X[b + k_]); SB(); \
                   _Pragma("unroll") for (int k_ = 0; k_ < 4; ++k_) tt[k_] = 1.f + tt[k_]; SB(); }
#define RMC(X, b, P) { _Pragma("unroll") for (int k_ = 0; k_ < 4; ++k_) tt[k_] = __builtin_amdgcn_rcpf(tt[k_]); SB(); \
                   _Pragma("unroll") for (int k_ = 0; k_ < 4; ++k_) tt[k_] = vmul(tt[k_], X[b + k_]); SB(); \
                   P[(b) >> 3][((b) >> 1) & 3] = cvtpk(tt[0], tt[1]); P[(b) >> 3][(((b) >> 1) & 3) + 1] = cvtpk(tt[2], tt[3]); SB(); }
#define BF(x) __builtin_bit_cast(bf16x8, x)
    {
      const u32x2 ds2 = ldl<u32x2>(a_de); const u32 de = ds2.x, dsb = ds2.y >> 16; const u32x4 sv = ldsrc(a_sr - 512);
      wait_dma(1, wnt);
#pragma unroll
      for (int s = 0; s < 4; ++s) As[s] = ldl<bf16x8>(sa[s]);
      front(dsb, ~0u); lastc = (lastn - tg) >> 2;
      rdwin();
      asm volatile("s_waitcnt lgkmcnt(0)" ::: "memory");
      issue(2, wnt, sv, slot0);
      OH.w |= de & dem;
      X0 = MFMA(As[0], W[0], ZERO); X0 = MFMA(As[1], W[2], X0); X0 = MFMA(As[2], W[4], X0); X0 = MFMA(As[3], W[6], X0); X0 = MFMA(BF(OH), Pb0, X0);
    }
    auto body = [&](auto uc, u32 t) {
      constexpr int U = decltype(uc)::value;
      const u32x4 dsw = ldsrc(a_v + 12 + 512 * U);
      const u32x2 ds2 = ldl<u32x2>(a_de + 512 * (U + 1)); const u32 de = ds2.x, dsb = ds2.y >> 16;
      const u32x4 sv = ldsrc(a_sr + 512 * U);
      SB();
      X1 = MFMA(BF(OH), Pb1, ZERO); SB();
      EA(X0, 0)
      X1 = MFMA(As[0], W[1], X1); SB();
      RMC(X0, 0, P0)
      X1 = MFMA(As[1], W[3], X1); SB();
      EA(X0, 4)
      X1 = MFMA(As[2], W[5], X1); SB();
      RMC(X0, 4, P0)
      X1 = MFMA(As[3], W[7], X1); SB();
      wait_dma(t + 2, wnt);
#pragma unroll
      for (int s = 0; s < 4; ++s) As[s] = ldl<bf16x8>(sa[s] + 4096 * (U ^ 1));
      SB();
      front(dsb, t + 1 < wnt ? ~0u : 0u);
      u32 sel[4];
#pragma unroll
      for (int a = 0; a < 4; ++a) sel[a] = (dsw[a] >> 16) == tgt ? a_v : zv;
      SB();
      EA(X0, 8) RMC(X0, 8, P0)
      asm volatile("s_waitcnt lgkmcnt(0)" ::: "memory");
      issue(t + 3, wnt, sv, slot0 + 4096 * (U ^ 1));
      rdwin();
      s16x4 Bq[4];
#pragma unroll
      for (int a = 0; a < 4; ++a) Bq[a] = ldtr(sel[a] + 128 * a + 512 * U);
      SB();
      EA(X0, 12) RMC(X0, 12, P0)
      X0 = MFMA(As[0], W[0], ZERO); SB();
      EA(X1, 0)
      X0 = MFMA(As[1], W[2], X0); SB();
      RMC(X1, 0, P1)
      X0 = MFMA(As[2], W[4], X0); SB();
      EA(X1, 4)
      X0 = MFMA(As[3], W[6], X0); SB();
      RMC(X1, 4, P1)
      OH.w |= de & dem; SB();
      X0 = MFMA(BF(OH), Pb0, X0); SB();
      EA(X1, 8)
      bf16x8 B0 = cat8(Bq[0], Bq[1]), B1 = cat8(Bq[2], Bq[3]);
      Y0 = MFMA(BF(P0[0]), B0, Y0); SB();
      RMC(X1, 8, P1)
      Y0 = MFMA(BF(P0[1]), B1, Y0); SB();
      EA(X1, 12)
      Y1 = MFMA(BF(P1[0]), B0, Y1); SB();
      RMC(X1, 12, P1)
      Y1 = MFMA(BF(P1[1]), B1, Y1); SB();
      if (lastc >= base + 8) do {
        flush(); base += 8; tgt += 32;
        asm volatile("" : "+s"(base), "+v"(tgt));
#pragma unroll
        for (int a = 0; a < 4; ++a) Bq[a] = ldtr(((dsw[a] >> 16) == tgt ? a_v : zv) + 128 * a + 512 * U);
        B0 = cat8(Bq[0], Bq[1]); B1 = cat8(Bq[2], Bq[3]);
        Y0 = MFMA(BF(P0[0]), B0, ZERO); Y1 = MFMA(BF(P1[0]), B0, ZERO);
        Y0 = MFMA(BF(P0[1]), B1, Y0); Y1 = MFMA(BF(P1[1]), B1, Y1);
      } while (lastc >= base + 8);
      lastc = (lastn - tg) >> 2;
    };
    for (u32 t = 0;; t += 2) {
      body(IC<0>{}, t);
      if (t + 1 >= wnt) break;
      body(IC<1>{}, t + 1);
      if (t + 2 >= wnt) break;
      a_de += 1024; a_v += 1024; a_sr += 1024;
    }
  }
  STAMP(16);
  {
    dma_rows(slot0);
    u32 sv; const char* g = (const char*)wh + w * 2048; const u32 vo = (lane * 16) ^ ((lane >> 4 & 1) << 6);
    asm volatile("s_mov_b32 %0, m0\n\ts_mov_b32 m0, %3\n\ts_nop 0\n\tglobal_load_lds_dwordx4 %1, %2\n\tglobal_load_lds_dwordx4 %1, %2 offset:1024\n\ts_mov_b32 m0, %0"
                 : "=&s"(sv) : "v"(vo), "s"(g), "s"(slot0 + 4096) : "memory");
  }
  if (base < wa1) { flush(); base += 8; }
  if (base < wa1) {
#pragma unroll
    for (int r = 0; r < 16; ++r) { Y0[r] = 0.f; Y1[r] = 0.f; }
    do { flush(); base += 8; } while (base < wa1);
  }
  if (bad >= 4 * XW && lane == 0) stl<u32>(l0 + L_MISC + 36, 1u);
  STAMP(17);
  asm volatile("s_waitcnt vmcnt(0) lgkmcnt(0)\n\ts_barrier" ::: "memory");
  STAMP(18);
  slow = __builtin_amdgcn_readfirstlane(ldl<u32>(l0 + L_MISC + 36)) != 0;

  if (!slow) {
  if (w < 7) {
    const u32 nl = 32 * w + e32, mrow = l0 + L_HM + min(nl, (u32)NPB) * RS + 16 * hh;
    f32x16 A0, A1, C0, C1;
#pragma unroll
    for (int r = 0; r < 16; ++r) { A0[r] = 0.f; A1[r] = 0.f; C0[r] = 0.f; C1[r] = 0.f; }
#pragma unroll
    for (int s = 0; s < 4; ++s) {
      const bf16x8 hv = ldl<bf16x8>(sa[s]), mb = ldl<bf16x8>(mrow + 32 * s);
      const u32 wa = l0 + L_STG + 4096 + wtr + 64 * (r4 >> 1), wb = l0 + L_STG + 4096 + wtr + 64 - 64 * (r4 >> 1);
      const bf16x8 w0 = cat8(ldtr(wa + s * 8192), ldtr(wa + s * 8192 + 512)), w1 = cat8(ldtr(wb + s * 8192), ldtr(wb + s * 8192 + 512));
      const bf16x8 w2 = cat8(ldtr(wa + (4 + s) * 8192), ldtr(wa + (4 + s) * 8192 + 512)), w3 = cat8(ldtr(wb + (4 + s) * 8192), ldtr(wb + (4 + s) * 8192 + 512));
      A0 = MFMA(w0, hv, A0); A1 = MFMA(w1, hv, A1); C0 = MFMA(w2, mb, C0); C1 = MFMA(w3, mb, C1);
    }
#pragma unroll
    for (int ft = 0; ft < 2; ++ft)
#pragma unroll
      for (int q = 0; q < 4; ++q) {
        const u32 a = slot0 + e32 * 128 + 16 * ((4 * ft + q) ^ swz) + 8 * hh;
        const u32x2 hv = ldl<u32x2>(a);
        float o[4];
#pragma unroll
        for (int k = 0; k < 4; ++k) { const float z = ft ? A1[4 * q + k] - LN2 * C1[4 * q + k] : A0[4 * q + k] - LN2 * C0[4 * q + k]; o[k] = silu_f(z); }
        stl<u32x2>(a, u32x2{cvtpk(lo2f(hv.x) + o[0], hi2f(hv.x) + o[1]), cvtpk(lo2f(hv.y) + o[2], hi2f(hv.y) + o[3])});
      }
    asm volatile("s_waitcnt lgkmcnt(0)" ::: "memory");
#pragma unroll
    for (int a = 0; a < 4; ++a) {
      const u32 row = 8 * a + (lane >> 3), nr = 32 * w + row;
      const u32x4 v = ldl<u32x4>(slot0 + a * 1024 + lane * 16);
      if (nr < NPB && n0 + nr < (u32)N) *(u32x4*)((char*)out + (size_t)(n0 + nr) * 128 + ((a & 1) ? dch1 : dch0)) = v;
    }
  } else {
    const int nn = min(NPB, N - n0);
    u32* po = (u32*)(out + (size_t)N * 64 + (size_t)n0 * 3);
    for (int i = lane; 2 * i < 3 * nn; i += 64) {
      const int e0 = 2 * i, e1 = e0 + 1, a0 = e0 / 3, a1 = e1 / 3, c0 = e0 - 3 * a0, c1 = e1 - 3 * a1;
      const float v0 = ldl<float>(l0 + L_PT + 16 * a0 + 4 * c0) + ldl<float>(l0 + L_PA + 16 * a0 + 4 * c0);
      const float v1 = ldl<float>(l0 + L_PT + 16 * a1 + 4 * c1) + ldl<float>(l0 + L_PA + 16 * a1 + 4 * c1);
      po[i] = cvtpk(v0, v1);
    }
  }
  STAMP(19);
  }
  }
  if (slow) slow_bucket(h, pos, ea, src, dst, we, wx, wh, out, N, E, bk, l0);
  STAMP_DUMP(160u << 20, bk * 8 + w);
}

__global__ void gen_zero(float* p, int n) { int i = blockIdx.x * blockDim.x + threadIdx.x; if (i < n) p[i] = 0.f; }
__global__ void gen_edge(const u16* h, const u16* pos, const u16* ea, const int* src, const int* dst, const u16* we, const u16* wx, float* mi, float* pd, int E) {
  const int lane = threadIdx.x & 63, w = threadIdx.x >> 6, nw = blockDim.x >> 6;
  const float wxf = bf2f(wx[lane]);
  for (int e = blockIdx.x * nw + w; e < E; e += gridDim.x * nw) {
    const int s = src[e], d = dst[e];
    float df[3], d2 = 0.f;
#pragma unroll
    for (int k = 0; k < 3; ++k) { df[k] = lo2f(cvtpk(bf2f(pos[(size_t)d * 3 + k]) - bf2f(pos[(size_t)s * 3 + k]), 0.f)); d2 += df[k] * df[k]; }
    d2 = lo2f(cvtpk(d2, 0.f));
    const float hd = bf2f(h[(size_t)d * 64 + lane]), hs = bf2f(h[(size_t)s * 64 + lane]);
    float z = 0.f;
#pragma unroll 4
    for (int k = 0; k < 64; ++k) z += __shfl(hd, k) * bf2f(we[k * 64 + lane]) + __shfl(hs, k) * bf2f(we[(64 + k) * 64 + lane]);
    z += d2 * bf2f(we[128 * 64 + lane]) + bf2f(ea[e]) * bf2f(we[129 * 64 + lane]);
    const float m = lo2f(cvtpk(silu_f(z), 0.f));
    float cs = m * wxf;
#pragma unroll
    for (int o = 32; o > 0; o >>= 1) cs += __shfl_xor(cs, o);
    atomicAdd(&mi[(size_t)d * 64 + lane], m);
    if (lane < 3) atomicAdd(&pd[(size_t)d * 3 + lane], (lane == 0 ? df[0] : lane == 1 ? df[1] : df[2]) * cs);
  }
}
__global__ void gen_node(const u16* h, const u16* pos, const u16* wh, const float* mi, const float* pd, u16* out, int N) {
  const int lane = threadIdx.x & 63, w = threadIdx.x >> 6, nw = blockDim.x >> 6;
  for (int n = blockIdx.x * nw + w; n < N; n += gridDim.x * nw) {
    const float hv = bf2f(h[(size_t)n * 64 + lane]), mv = lo2f(cvtpk(mi[(size_t)n * 64 + lane], 0.f));
    float z = 0.f;
#pragma unroll 4
    for (int k = 0; k < 64; ++k) z += __shfl(hv, k) * bf2f(wh[k * 64 + lane]) + __shfl(mv, k) * bf2f(wh[(64 + k) * 64 + lane]);
    out[(size_t)n * 64 + lane] = (u16)cvtpk(hv + silu_f(z), 0.f);
    if (lane < 3) out[(size_t)N * 64 + (size_t)n * 3 + lane] = (u16)cvtpk(bf2f(pos[(size_t)n * 3 + lane]) + pd[(size_t)n * 3 + lane], 0.f);
  }
}

extern "C" void kernel_launch(void* const* d_in, const int* in_sizes, int n_in, void* d_out, int out_size, void* d_ws, size_t ws_size, hipStream_t stream) {
  const u16* h = (const u16*)d_in[0];
  const u16* pos = (const u16*)d_in[1];
  const u16* ea = (const u16*)d_in[2];
  const int* src = (const int*)d_in[3];
  const int* dst = (const int*)d_in[4];
  const u16* we = (const u16*)d_in[5];
  const u16* wx = (const u16*)d_in[6];
  const u16* wh = (const u16*)d_in[7];
  const int N = in_sizes[0] / 64, E = in_sizes[3];
  const int CS = ((E + NCH - 1) / NCH + 3) & ~3;
  const bool fast = N >= 2 && N <= NPB * NBK && (N * 6) % 64 != 0 && (E & 3) == 0 && E > 0 && CS <= 4096 && W_REC + (size_t)NCH * CS * 8 <= ws_size;
  if (fast) {
    (void)hipFuncSetAttribute((const void*)egnn_fused, hipFuncAttributeMaxDynamicSharedMemorySize, L_END);
    egnn_fused<<<NBK, 512, L_END, stream>>>(h, pos, ea, src, dst, we, wx, wh, (char*)d_ws, (u16*)d_out, N, E, CS);
  } else {
    float* mi = (float*)d_ws; float* pd = mi + (size_t)N * 64;
    const int nz = N * 67;
    gen_zero<<<(nz + 255) / 256, 256, 0, stream>>>(mi, nz);
    gen_edge<<<2048, 256, 0, stream>>>(h, pos, ea, src, dst, we, wx, mi, pd, E);
    gen_node<<<1024, 256, 0, stream>>>(h, pos, wh, mi, pd, (u16*)d_out, N);
  }
}
```

```cpp
#include <hip/hip_runtime.h>
#include <hip/hip_bf16.h>
#include <stdint.h>

typedef unsigned short u16;
typedef unsigned u32;
typedef short bf16x8 __attribute__((ext_vector_type(8)));
typedef short s16x4 __attribute__((ext_vector_type(4)));
typedef float f32x16 __attribute__((ext_vector_type(16)));
typedef float f32x4 __attribute__((ext_vector_type(4)));
typedef unsigned u32x4 __attribute__((ext_vector_type(4)));
typedef unsigned u32x2 __attribute__((ext_vector_type(2)));
#define LAS __attribute__((address_space(3)))

constexpr int NPB = 196;
constexpr int NBK = 256;
constexpr int NCH = 256;
constexpr int MAXT = 112;
constexpr float LOG2E = 1.4426950408889634f, LN2 = 0.6931471805599453f;

constexpr int NT1 = MAXT + 1;
constexpr int RS = 144;
constexpr int XW = 14;
constexpr int L_STG = 0;
constexpr int L_R = 65536;
constexpr int L_HM = L_R + NT1 * 512;
constexpr int L_PT = L_HM + 6 * 32 * RS + 1024;
constexpr int L_PA = L_PT + NPB * 16;
constexpr int L_CNT = L_PA + NPB * 16;
constexpr int L_WX = L_CNT + 1024;
constexpr int L_WR = L_WX + 128;
constexpr int L_OH = L_WR + 256;
constexpr int L_Z = L_OH + 256;
constexpr int L_MISC = L_Z + 1024;
constexpr int L_END = L_MISC + 64;
constexpr int L_SREC = L_STG;
constexpr int L_WRAW = L_STG + 32768;
constexpr int L_HC = L_STG + 49152;
constexpr int L_HS = L_HC + 2048;

constexpr size_t W_EP = 0;
constexpr size_t W_TAB = 4096;
constexpr size_t W_REC = W_TAB + NCH * NBK * 16;

static __device__ __forceinline__ float bf2f(u32 v) { return __uint_as_float(v << 16); }
typedef __bf16 bf16x2_t __attribute__((ext_vector_type(2)));
typedef float f32x2 __attribute__((ext_vector_type(2)));
static __device__ __forceinline__ u32 cvtpk(float lo, float hi) { return __builtin_bit_cast(u32, __builtin_convertvector(f32x2{lo, hi}, bf16x2_t)); }
static __device__ __forceinline__ float lo2f(u32 v) { return __uint_as_float(v << 16); }
static __device__ __forceinline__ float hi2f(u32 v) { return __uint_as_float(v & 0xffff0000u); }
template <int CTRL, int RM = 0xf> static __device__ __forceinline__ u32 dpp0(u32 x) { return (u32)__builtin_amdgcn_update_dpp(0, (int)x, CTRL, RM, 0xf, true); }
static __device__ __forceinline__ u32 wave_incl_scan(u32 x) {
  x += dpp0<0x111>(x); x += dpp0<0x112>(x); x += dpp0<0x114>(x); x += dpp0<0x118>(x);
  x += dpp0<0x142, 0xa>(x); x += dpp0<0x143, 0xc>(x); return x; }
#define MFMA(a, b, c) __builtin_amdgcn_mfma_f32_32x32x16_bf16(a, b, c, 0, 0, 0)
#define SB() __builtin_amdgcn_sched_barrier(0)
#define LBAR() asm volatile("s_waitcnt lgkmcnt(0)\n\ts_barrier" ::: "memory")
template <class T> static __device__ __forceinline__ T ldl(u32 a) { return *(LAS const T*)(size_t)a; }
template <class T> static __device__ __forceinline__ void stl(u32 a, T v) { *(LAS T*)(size_t)a = v; }
static __device__ __forceinline__ s16x4 ldtr(u32 a) { typedef short v4 __attribute__((ext_vector_type(4)));
  return __builtin_bit_cast(s16x4, __builtin_amdgcn_ds_read_tr16_b64_v4i16((LAS v4*)(size_t)a)); }
static __device__ __forceinline__ float silu_f(float z) { return z * __builtin_amdgcn_rcpf(1.f + __builtin_amdgcn_exp2f(-LOG2E * z)); }

struct Dma { const char* b0; const char* b1; const char* b2; const char* b3; };
static __device__ __forceinline__ void dma4(u32 v0, u32 v1, u32 v2, u32 v3, const Dma& d, u32 lds) {
  u32 sv;
  asm volatile("s_mov_b32 %0, m0\n\ts_mov_b32 m0, %9\n\ts_nop 0\n\t"
               "global_load_lds_dwordx4 %1, %5\n\t"
               "global_load_lds_dwordx4 %2, %6 offset:1024\n\t"
               "global_load_lds_dwordx4 %3, %7 offset:2048\n\t"
               "global_load_lds_dwordx4 %4, %8 offset:3072\n\t"
               "s_mov_b32 m0, %0"
               : "=&s"(sv) : "v"(v0), "v"(v1), "v"(v2), "v"(v3), "s"(d.b0), "s"(d.b1), "s"(d.b2), "s"(d.b3), "s"(lds) : "memory");
}

static __device__ __forceinline__ void slow_bucket(const u16* __restrict__ h, const u16* __restrict__ pos, const u16* __restrict__ ea, const int* __restrict__ src,
                                                const int* __restrict__ dst, const u16* __restrict__ we, const u16* __restrict__ wx,
                                                const u16* __restrict__ wh, u16* __restrict__ out, int N, int E, int bk, u32 l0) {
  const int tid = threadIdx.x, lane = tid & 63, w = tid >> 6, n0 = bk * NPB;
  LAS float* macc = (LAS float*)(size_t)(l0 + L_STG);
  LAS float* pacc = (LAS float*)(size_t)(l0 + L_PA);
  __syncthreads();
  for (int i = tid; i < NPB * 64; i += 512) macc[i] = 0.f;
  for (int i = tid; i < NPB * 4; i += 512) pacc[i] = 0.f;
  __syncthreads();
  const float wxf = bf2f(wx[lane]);
  for (int eb = 64 * w; eb < E; eb += 512) {
    const int dv = eb + lane < E ? dst[eb + lane] : -1;
    unsigned long long m = __builtin_amdgcn_ballot_w64((u32)(dv - n0) < (u32)NPB && dv >= 0);
    while (m) {
      const int l = __builtin_ctzll(m); m &= m - 1;
      const int e = eb + l, d = __builtin_amdgcn_readlane(dv, l), s = src[e], dl = d - n0;
      float df[3], d2 = 0.f;
#pragma unroll
      for (int k = 0; k < 3; ++k) { df[k] = lo2f(cvtpk(bf2f(pos[d * 3 + k]) - bf2f(pos[s * 3 + k]), 0.f)); d2 += df[k] * df[k]; }
      d2 = lo2f(cvtpk(d2, 0.f));
      const float hd = bf2f(h[(size_t)d * 64 + lane]), hs = bf2f(h[(size_t)s * 64 + lane]);
      float z = 0.f;
#pragma unroll 4
      for (int k = 0; k < 64; ++k) z += __shfl(hd, k) * bf2f(we[k * 64 + lane]) + __shfl(hs, k) * bf2f(we[(64 + k) * 64 + lane]);
      z += d2 * bf2f(we[128 * 64 + lane]) + bf2f(ea[e]) * bf2f(we[129 * 64 + lane]);
      const float mm = lo2f(cvtpk(silu_f(z), 0.f));
      float cs = mm * wxf;
#pragma unroll
      for (int o = 32; o > 0; o >>= 1) cs += __shfl_xor(cs, o);
      atomicAdd((float*)&macc[dl * 64 + lane], mm);
      if (lane < 3) atomicAdd((float*)&pacc[dl * 4 + lane], (lane == 0 ? df[0] : lane == 1 ? df[1] : df[2]) * cs);
    }
  }
  __syncthreads();
  for (int nl = w; nl < NPB; nl += 8) {
    const int n = n0 + nl; if (n >= N) break;
    const float hv = bf2f(h[(size_t)n * 64 + lane]), mv = lo2f(cvtpk(macc[nl * 64 + lane], 0.f));
    float z = 0.f;
#pragma unroll 4
    for (int k = 0; k < 64; ++k) z += __shfl(hv, k) * bf2f(wh[k * 64 + lane]) + __shfl(mv, k) * bf2f(wh[(64 + k) * 64 + lane]);
    out[(size_t)n * 64 + lane] = (u16)cvtpk(hv + silu_f(z), 0.f);
    if (lane < 3) out[(size_t)N * 64 + (size_t)n * 3 + lane] = (u16)cvtpk(bf2f(pos[n * 3 + lane]) + pacc[nl * 4 + lane], 0.f);
  }
}
static __device__ __forceinline__ u32 umul24(u32 a, u32 b) { return (u32)__umul24(a, b); }
static __device__ __forceinline__ u32 mad16lo(u32 a, u32 b, u32 c) { u32 r; asm("v_mad_u32_u16 %0, %1, %2, %3" : "=v"(r) : "v"(a), "s"(b), "v"(c)); return r; }
static __device__ __forceinline__ u32 mad16hi(u32 a, u32 b, u32 c) { u32 r; asm("v_mad_u32_u16 %0, %1, %2, %3 op_sel:[1,0,0,0]" : "=v"(r) : "v"(a), "s"(b), "v"(c)); return r; }
static __device__ __forceinline__ bf16x8 cat8(s16x4 a, s16x4 b) { return __builtin_shufflevector(a, b, 0, 1, 2, 3, 4, 5, 6, 7); }
extern "C" __device__ float llvm_fmul_legacy(float, float) __asm("llvm.amdgcn.fmul.legacy");
static __device__ __forceinline__ float vmul(float a, float b) { return llvm_fmul_legacy(a, b); }
static __device__ __forceinline__ void wait_dma(u32 a, u32 b) {
  asm volatile("s_cmp_lt_u32 %0, %1\n\ts_cbranch_scc1 1f\n\ts_waitcnt vmcnt(0)\n1:\n\ts_waitcnt vmcnt(4)" :: "s"(a), "s"(b) : "scc", "memory"); }
static __device__ __forceinline__ void dma4_if(u32 a, u32 b, u32 v0, u32 v1, u32 v2, u32 v3, const Dma& d, u32 lds) {
  u32 sv;
  asm volatile("s_cmp_lt_u32 %10, %11\n\ts_cbranch_scc0 1f\n\t"
               "s_mov_b32 %0, m0\n\ts_mov_b32 m0, %9\n\ts_nop 0\n\t"
               "global_load_lds_dwordx4 %1, %5\n\t"
               "global_load_lds_dwordx4 %2, %6 offset:1024\n\t"
               "global_load_lds_dwordx4 %3, %7 offset:2048\n\t"
               "global_load_lds_dwordx4 %4, %8 offset:3072\n\t"
               "s_mov_b32 m0, %0\n1:"
               : "=&s"(sv) : "v"(v0), "v"(v1), "v"(v2), "v"(v3), "s"(d.b0), "s"(d.b1), "s"(d.b2), "s"(d.b3), "s"(lds), "s"(a), "s"(b) : "scc", "memory");
}
template <int V> struct IC { static constexpr int value = V; };

constexpr u32 SPIN_MAX = 1u << 15;
typedef __attribute__((address_space(1))) unsigned long long gu64;

__global__ void __launch_bounds__(512, 2) egnn_fused(const u16* __restrict__ h, const u16* __restrict__ pos, const u16* __restrict__ ea, const int* __restrict__ src,
                                                     const int* __restrict__ dst, const u16* __restrict__ we, const u16* __restrict__ wx, const u16* __restrict__ wh,
                                                     char* __restrict__ ws, u16* __restrict__ out, int N, int E, int CS) {
  extern __shared__ __attribute__((aligned(16))) char smem[];
  const u32 l0 = (u32)(size_t)(LAS char*)smem;
  const int tid = threadIdx.x, lane = tid & 63;
  const int w = __builtin_amdgcn_readfirstlane(tid >> 6);
  const int c = blockIdx.x;
  const int bk = (blockIdx.x & 7) * 32 + (blockIdx.x >> 3);
  const int n0 = bk * NPB;

  const int grp = lane >> 4, j16 = lane & 15, e32 = lane & 31, hh = lane >> 5;
  const int e0 = c * CS + 4 * tid;
  const bool act0 = 4 * tid < CS && e0 < E, act1 = 4 * tid + 2048 < CS && e0 + 2048 < E;
  const __amdgpu_buffer_rsrc_t rdst = __builtin_amdgcn_make_buffer_rsrc((void*)dst, 0, E * 4, 0x00020000), rsrc_ = __builtin_amdgcn_make_buffer_rsrc((void*)src, 0, E * 4, 0x00020000);
  const __amdgpu_buffer_rsrc_t rea = __builtin_amdgcn_make_buffer_rsrc((void*)ea, 0, E * 2, 0x00020000);
  const u32 eo0 = act0 ? (u32)e0 : 0x1fffff00u, eo1 = act1 ? (u32)e0 + 2048 : 0x1fffff00u;
  u32x4 d4[2], s4[2]; u32x2 a4[2];
  d4[0] = __builtin_bit_cast(u32x4, __builtin_amdgcn_raw_buffer_load_b128(rdst, eo0 * 4, 0, 2));
  d4[1] = __builtin_bit_cast(u32x4, __builtin_amdgcn_raw_buffer_load_b128(rdst, eo1 * 4, 0, 2));
  s4[0] = __builtin_bit_cast(u32x4, __builtin_amdgcn_raw_buffer_load_b128(rsrc_, eo0 * 4, 0, 2));
  s4[1] = __builtin_bit_cast(u32x4, __builtin_amdgcn_raw_buffer_load_b128(rsrc_, eo1 * 4, 0, 2));
  a4[0] = __builtin_bit_cast(u32x2, __builtin_amdgcn_raw_buffer_load_b64(rea, eo0 * 2, 0, 2));
  a4[1] = __builtin_bit_cast(u32x2, __builtin_amdgcn_raw_buffer_load_b64(rea, eo1 * 2, 0, 2));
  const unsigned long long epv = __hip_atomic_load((gu64*)(ws + W_EP), __ATOMIC_RELAXED, __HIP_MEMORY_SCOPE_AGENT);
  const __amdgpu_buffer_rsrc_t rcode = __builtin_amdgcn_make_buffer_rsrc((void*)(__builtin_amdgcn_s_getpc() & ~127ull), 0, 144 * 128, 0x00020000);
  const u32 cpt = __builtin_amdgcn_raw_buffer_load_b32(rcode, blockIdx.x < 8 && tid >= 368 ? (u32)(tid - 368) * 128 : 0x7fffff00u, 0, 0);
  u32x4 wt[2];
#pragma unroll
  for (int i = 0; i < 2; ++i) wt[i] = ((const u32x4*)we)[tid + 512 * i];
  const u32 wxr = wx[16 * ((tid >> 4) & 3) + (tid & 3) + 8 * ((tid >> 2) & 1) + 4 * ((tid >> 3) & 1)];
  const u32 wrr = we[128 * 64 + (tid & 127)];
  const int nmax = N - 1;
  const __amdgpu_buffer_rsrc_t rpos = __builtin_amdgcn_make_buffer_rsrc((void*)pos, 0, N * 6 + 2, 0x00020000);
  const u32x2 ptile = __builtin_bit_cast(u32x2, __builtin_amdgcn_raw_buffer_load_b64(rpos, min(n0 + tid, nmax) * 6, 0, 0));
  const Dma dh = {(const char*)h, (const char*)h - 1024, (const char*)h - 2048, (const char*)h - 3072};
  const u32 dch0 = 16 * ((lane & 7) ^ (lane >> 4)), dch1 = dch0 ^ 64;
  const u32 hbs = l0 + L_HM + 32 * RS * w;
  auto dma_rows = [&](u32 slot) {
    const u32 rb = (u32)(n0 + 32 * w) + (lane >> 3), nm = (u32)nmax;
    dma4(min(rb, nm) * 128 + dch0, min(rb + 8, nm) * 128 + dch1, min(rb + 16, nm) * 128 + dch0, min(rb + 24, nm) * 128 + dch1, dh, slot); };
  u32x4 hq[4];
  const __amdgpu_buffer_rsrc_t rh = __builtin_amdgcn_make_buffer_rsrc((void*)h, 0, N * 128, 0x00020000);
  auto ldh = [&](int i) { const u32 nl = 32 * w + 8 * i + (lane >> 3);
    hq[i] = __builtin_bit_cast(u32x4, __builtin_amdgcn_raw_buffer_load_b128(rh, nl < 200 ? min(n0 + nl, (u32)nmax) * 128 + 16 * (lane & 7) : 0x7fffff00u, 0, 0)); };
#pragma unroll
  for (int i = 0; i < 4; ++i) ldh(i);
  if (tid < 256) { stl<u32>(l0 + L_CNT + 4 * tid, 0u); stl<u32x2>(l0 + L_HC + 8 * tid, u32x2{0, 0}); }
  if (tid < 16) stl<u32>(l0 + L_MISC + 4 * tid, tid < 8 ? 0xffffffffu : 0u);
  if (tid < 256) stl<u32>(l0 + L_Z + 4 * tid, 0u);
  const u32 lst = l0 + L_R + 2048 * w;
  stl<u32x4>(lst + 32 * lane, u32x4{8, 8, 8, 8}); stl<u32x4>(lst + 32 * lane + 16, u32x4{8, 8, 8, 8});
  if (tid < NPB) stl<f32x4>(l0 + L_PA + 16 * tid, f32x4{0, 0, 0, 0});
  if (tid < 64) { const int u = tid < 36 ? tid : tid - 36, e = u >> 2, lim = tid < 36 ? 8 : 6;
    stl<u32>(l0 + L_OH + 4 * tid, (e < lim && (e >> 1) == (u & 3)) ? ((e & 1) ? 0x3f800000u : 0x3f80u) : 0u); }
  LBAR();
  const u32 tag = __builtin_amdgcn_readfirstlane((u32)epv) + 1u, ephi = __builtin_amdgcn_readfirstlane((u32)(epv >> 32)), tag6 = ephi & 63u;
  const u32 tagx = (tag >> 16 ^ tag6) & 63u, tagy = tag & 0xffffu;
  u32 b[8], r[8]; u32x2 rc[8];
#pragma unroll
  for (int j = 0; j < 8; ++j) {
    b[j] = umul24(d4[j >> 2][j & 3], 85599u) >> 24;
    const u32 eav = (j & 1) ? (a4[j >> 2][(j >> 1) & 1] & 0xffff0000u) : (a4[j >> 2][(j >> 1) & 1] << 16);
    rc[j] = u32x2{s4[j >> 2][j & 3] | (d4[j >> 2][j & 3] - umul24(b[j], NPB)) << 18 | tagx << 26, eav | tagy};
  }
#define HIST(j) r[j] = (u32)__hip_atomic_fetch_add((LAS unsigned long long*)(size_t)(l0 + L_HC + 8 * b[j]), (unsigned long long)(rc[j].x + rc[j].y) << 32 | 1u, __ATOMIC_RELAXED, __HIP_MEMORY_SCOPE_WORKGROUP);
  if (act0) {
#pragma unroll
    for (int j = 0; j < 4; ++j) HIST(j)
  }
  if (act1) {
#pragma unroll
    for (int j = 4; j < 8; ++j) HIST(j)
  }
#pragma unroll
  for (int i = 0; i < 2; ++i) {
    const u32 q = tid + 512 * i;
    u32x4 o;
#pragma unroll
    for (int k = 0; k < 4; ++k) o[k] = cvtpk(lo2f(wt[i][k]) * -LOG2E, hi2f(wt[i][k]) * -LOG2E);
    stl<u32x4>(l0 + L_WRAW + ((q * 16) ^ ((q >> 4 & 1) << 6)), o);
  }
  if (tid < 64) stl<u16>(l0 + L_WX + 2 * tid, (u16)wxr);
  if (tid < 128) stl<u16>(l0 + L_WR + 2 * tid, (u16)cvtpk(bf2f(wrr) * -LOG2E, 0.f));
  if (tid < NPB) stl<f32x4>(l0 + L_PT + 16 * tid, f32x4{lo2f(ptile.x), hi2f(ptile.x), lo2f(ptile.y), 0.f});
  asm volatile("" :: "v"(s4[0]), "v"(s4[1]), "v"(a4[0]), "v"(a4[1]), "v"(ptile), "v"(epv));
  LBAR();
  {
    const u32x4 c0 = ldl<u32x4>(l0 + L_HC + 32 * lane), c1 = ldl<u32x4>(l0 + L_HC + 32 * lane + 16);
    const u32x4 cv = {c0.x, c0.z, c1.x, c1.z};
    const u32 s1 = cv.x + cv.y, s2 = s1 + cv.z, s3 = s2 + cv.w;
    const u32 ex = wave_incl_scan(s3) - s3;
    const u32x4 st = {ex, ex + cv.x, ex + s1, ex + s2};
    stl<u32x4>(l0 + L_HS + 16 * lane, st);
    if (w < 4) {
      const __amdgpu_buffer_rsrc_t rtab = __builtin_amdgcn_make_buffer_rsrc((void*)(ws + W_TAB), 0, NCH * NBK * 16, 0x00020000);
      const u32 tv = w == 0 ? st.x | cv.x << 13 : w == 1 ? st.y | cv.y << 13 : w == 2 ? st.z | cv.z << 13 : st.w | cv.w << 13;
      const u32 cs = w == 0 ? c0.y : w == 1 ? c0.w : w == 2 ? c1.y : c1.w;
      __builtin_amdgcn_raw_buffer_store_b128(u32x4{tv | tag6 << 26, tag, cs, tag}, rtab, ((4 * lane + w) * NCH + c) * 16, 0, 16);
    }
  }
  {
    u32 sp[8];
#pragma unroll
    for (int j = 0; j < 8; ++j) sp[j] = ldl<u32>(l0 + L_HS + 4 * b[j]);
#pragma unroll
    for (int j = 0; j < 8; ++j) if (j < 4 ? act0 : act1) stl<u32x2>(l0 + L_SREC + 8 * (sp[j] + r[j]), rc[j]);
  }
  LBAR();
  const __amdgpu_buffer_rsrc_t rrec = __builtin_amdgcn_make_buffer_rsrc((void*)(ws + W_REC), 0, NCH * CS * 8, 0x00020000);
  {
    const int n = min(CS, E - c * CS);
    u32x4 cp[4];
#pragma unroll
    for (int i = 0; i < 4; ++i) cp[i] = ldl<u32x4>(l0 + L_SREC + 16 * (tid + 512 * i));
#pragma unroll
    for (int i = 0; i < 4; ++i) { const int q = tid + 512 * i;
      __builtin_amdgcn_raw_buffer_store_b128(cp[i], rrec, 2 * q < n ? (c * CS + 2 * q) * 8 : 0x7ffffff0, 0, 16); }
  }
  if (w < 7) {
    stl<u32x4>(hbs + (lane >> 3) * 128 + dch0, hq[0]);
    if (w < 6) { stl<u32x4>(hbs + 1024 + (lane >> 3) * 128 + dch1, hq[1]); stl<u32x4>(hbs + 2048 + (lane >> 3) * 128 + dch0, hq[2]); stl<u32x4>(hbs + 3072 + (lane >> 3) * 128 + dch1, hq[3]); }
  }
  const u32 r4 = (lane >> 2) & 3, cofs = 32 * ((lane >> 4) & 1) + 8 * (lane & 3);
  const u32 wtr = (8 * hh + r4) * 128 + cofs;
  bf16x8 W[8], W1[8];
  {
    const u32 wa0_ = l0 + L_WRAW + wtr + 64 * (r4 >> 1), wa1_ = l0 + L_WRAW + wtr + 64 - 64 * (r4 >> 1);
#pragma unroll
    for (int i = 0; i < 8; ++i) { const u32 a = ((i & 1) ? wa1_ : wa0_) + (i >> 1) * 2048; W1[i] = cat8(ldtr(a), ldtr(a + 512)); W[i] = cat8(ldtr(a + 8192), ldtr(a + 8192 + 512)); }
  }
  if (n0 >= N) { asm volatile("s_waitcnt vmcnt(0)" ::: "memory"); return; }
  u32 ent, cks;
  {
    const char* tb = ws + W_TAB; const u32 vo = (bk * NCH + 32 * w + e32) * 16;
    u32x4 g; bool ok; u32 spins = 0;
    do {
      asm volatile("global_load_dwordx4 %0, %1, %2 sc1\n\ts_waitcnt vmcnt(0)" : "=&v"(g) : "v"(vo), "s"(tb) : "memory");
      ok = __builtin_amdgcn_ballot_w64(g.y != tag || g.w != tag || (g.x >> 26) != tag6) == 0;
      if (!ok) __builtin_amdgcn_s_sleep(1);
    } while (!ok && ++spins < SPIN_MAX);
    ent = ok && lane < 32 ? g.x : 0u; cks = lane < 32 ? 0u - g.z : 0u;
    if (!ok) stl<u32>(l0 + L_MISC + 36, 1u);
  }
  asm volatile("" :: "v"(cpt));
  u32 Tw, adr[8];
  {
    const u32 cn = (ent >> 13) & 0x1fff;
    u32 inc = cn; inc += dpp0<0x111>(inc); inc += dpp0<0x112>(inc); inc += dpp0<0x114>(inc); inc += dpp0<0x118>(inc); inc += dpp0<0x142, 0xa>(inc);
    Tw = __builtin_amdgcn_readlane(inc, 31);
    const u32 pre = inc - cn;
    const u32 delta = 8 * ((u32)(32 * w + lane) * (u32)CS + (ent & 0x1fff) - pre);
    const u32 dprev = (u32)__builtin_amdgcn_update_dpp(8, (int)delta, 0x138, 0xf, 0xf, false);
    if (lane < 32 && pre < 512) __hip_atomic_fetch_add((LAS u32*)(size_t)(lst + 4 * pre), delta - dprev, __ATOMIC_RELAXED, __HIP_MEMORY_SCOPE_WORKGROUP);
    const u32x4 m0 = ldl<u32x4>(lst + 32 * lane), m1 = ldl<u32x4>(lst + 32 * lane + 16);
    u32 a[8];
    a[0] = m0.x; a[1] = a[0] + m0.y; a[2] = a[1] + m0.z; a[3] = a[2] + m0.w; a[4] = a[3] + m1.x; a[5] = a[4] + m1.y; a[6] = a[5] + m1.z; a[7] = a[6] + m1.w;
    const u32 ex = wave_incl_scan(a[7]) - a[7];
    stl<u32x4>(lst + 32 * lane, u32x4{a[0] + ex, a[1] + ex, a[2] + ex, a[3] + ex});
    stl<u32x4>(lst + 32 * lane + 16, u32x4{a[4] + ex, a[5] + ex, a[6] + ex, a[7] + ex});
#pragma unroll
    for (int s = 0; s < 8; ++s) adr[s] = ldl<u32>(lst + 256 * s + 4 * lane);
  }
  if (Tw > 512) { stl<u32>(l0 + L_MISC + 36, 1u); Tw = 0; }
  const bool big = Tw > 448;
#define SLOTS(...) { _Pragma("unroll") for (int s = 0; s < 7; ++s) { __VA_ARGS__ } if (big) { constexpr int s = 7; __VA_ARGS__ } }
#define V(s) ((u32)(64 * (s) + lane) < Tw)
  u32x2 rec[8];
#define LDREC(off) __builtin_bit_cast(u32x2, __builtin_amdgcn_raw_buffer_load_b64(rrec, off, 0, 16))
#pragma unroll
  for (int s = 0; s < 8; ++s) { adr[s] = V(s) ? adr[s] : 0x7ffffff0u; rec[s] = LDREC(adr[s]); }
  const u32 swz = (e32 >> 1) & 7;
  if (w < 7) {
    f32x16 A0, A1;
#pragma unroll
    for (int q = 0; q < 16; ++q) { A0[q] = 0.f; A1[q] = 0.f; }
#pragma unroll
    for (int s = 0; s < 4; ++s) { const bf16x8 hb = ldl<bf16x8>(hbs + e32 * 128 + 16 * ((2 * s + hh) ^ swz)); A0 = MFMA(W1[2 * s], hb, A0); A1 = MFMA(W1[2 * s + 1], hb, A1); }
    const u32 nl = 32 * w + e32;
    if (nl <= NPB) {
      const u32 a = l0 + L_HM + nl * RS + 8 * hh;
#pragma unroll
      for (int q = 0; q < 4; ++q) {
        stl<u32x2>(a + 16 * q, u32x2{cvtpk(A0[4 * q], A0[4 * q + 1]), cvtpk(A0[4 * q + 2], A0[4 * q + 3])});
        stl<u32x2>(a + 64 + 16 * q, u32x2{cvtpk(A1[4 * q], A1[4 * q + 1]), cvtpk(A1[4 * q + 2], A1[4 * q + 3])});
      }
    }
  }
  __builtin_amdgcn_sched_barrier(0);
  u32x2 ps[8]; const u32 six = 6;
#define GATHER(s) { __builtin_amdgcn_sched_barrier(0); ps[s] = __builtin_bit_cast(u32x2, __builtin_amdgcn_raw_buffer_load_b64(rpos, mad16lo(rec[s].x, six, 0), 0, 0)); __builtin_amdgcn_sched_barrier(0); }
  {
    auto missing = [&]() {
      u32 acc = cks;
#pragma unroll
      for (int s = 0; s < 8; ++s) acc += rec[s].x + rec[s].y;
      return __builtin_amdgcn_readlane(wave_incl_scan(acc), 63) != 0; };
    if (Tw != 0 && missing()) {
      u32 spins = 0; bool miss;
      do {
        asm volatile("s_sleep 1" ::: "memory");
#pragma unroll
        for (int s = 0; s < 8; ++s) rec[s] = LDREC(adr[s]);
        miss = missing();
      } while (miss && ++spins < SPIN_MAX);
      if (miss) { stl<u32>(l0 + L_MISC + 36, 1u); Tw = 0; }
    }
  }
#pragma unroll
  for (int s = 0; s < 8; ++s) asm volatile("" : "+v"(rec[s].x), "+v"(rec[s].y));
  const u32 tg = tagx << 10;
  u32 rank[8];
  const u32 cntb = l0 + L_CNT - tg;
  GATHER(0)
  SLOTS(if (V(s)) rank[s] = __hip_atomic_fetch_add((LAS u32*)(size_t)(cntb + (rec[s].x >> 16)), 1u, __ATOMIC_RELAXED, __HIP_MEMORY_SCOPE_WORKGROUP);)
  GATHER(1)
  LBAR();
  if (blockIdx.x == 0 && tid == 0)
    __hip_atomic_store((gu64*)(ws + W_EP), (epv & 0xffffffff00000000ull) | tag, __ATOMIC_RELAXED, __HIP_MEMORY_SCOPE_AGENT);
  u32 wa0, wa1, wtb, wnt, wend; bool slow;
  {
    const u32x4 cv = ldl<u32x4>(l0 + L_CNT + 16 * lane);
    const u32 flag = ldl<u32>(l0 + L_MISC + 36);
    const u32 s1 = cv.x + cv.y, s2 = s1 + cv.z, s3 = s2 + cv.w;
    const u32 inc = wave_incl_scan(s3), ex = inc - s3;
    const u32 T = __builtin_amdgcn_readlane(inc, 63);
    const u32 off[4] = {ex, ex + cv.x, ex + s1, ex + s2};
    GATHER(2)
    const float rT = 8.f * __builtin_amdgcn_rcpf((float)max(T, 1u));
    u32 wv[4];
#pragma unroll
    for (int i = 0; i < 4; ++i) wv[i] = min(7u, (u32)((float)off[i] * rT));
    u32 prev = (u32)__builtin_amdgcn_update_dpp(-1, (int)wv[3], 0x138, 0xf, 0xf, false);
#pragma unroll
    for (int i = 0; i < 4; ++i) { const u32 n = 4 * lane + i;
      const u32 wsel = wv[i] != prev ? wv[i] : 12u;
      stl<u32>(l0 + L_MISC + 4 * (lane < NPB / 4 ? wsel : 12u), off[i] << 8 | n);
      prev = wv[i]; }
    u32 cut = min(ldl<u32>(l0 + L_MISC + 4 * (lane & 7)), T << 8 | NPB);
    if (j16 >= 8) cut = T << 8 | NPB;
    GATHER(3)
    cut = min(cut, (u32)__builtin_amdgcn_update_dpp(-1, (int)cut, 0x101, 0xf, 0xf, false));
    cut = min(cut, (u32)__builtin_amdgcn_update_dpp(-1, (int)cut, 0x102, 0xf, 0xf, false));
    cut = min(cut, (u32)__builtin_amdgcn_update_dpp(-1, (int)cut, 0x104, 0xf, 0xf, false));
    const u32 nxt = (u32)__builtin_amdgcn_update_dpp(-1, (int)cut, 0x101, 0xf, 0xf, false);
    u32 len = j16 < 8 ? (nxt >> 8) - (cut >> 8) : 0, nt = (len + 31) >> 5;
    u32 tb = nt; tb += dpp0<0x111>(tb); tb += dpp0<0x112>(tb); tb += dpp0<0x114>(tb);
    const u32 ttot = __builtin_amdgcn_readlane(tb, 7);
    tb -= nt;
    const u32 delta = 32 * tb - (cut >> 8);
#pragma unroll
    for (int i = 0; i < 4; ++i) { const u32 o = off[i] + (u32)__builtin_amdgcn_ds_bpermute(4 * wv[i], (int)delta);
      if (lane < NPB / 4) stl<u32>(l0 + L_PT + 64 * lane + 16 * i + 12, o); }
    GATHER(4)
    wa0 = __builtin_amdgcn_readlane(cut, w) & 0xff; wa1 = __builtin_amdgcn_readlane(nxt, w) & 0xff;
    wtb = __builtin_amdgcn_readlane(tb, w); wnt = __builtin_amdgcn_readlane(nt, w);
    wend = 32 * wtb + __builtin_amdgcn_readlane(len, w);
    slow = ttot > MAXT || __builtin_amdgcn_readfirstlane(flag) != 0;
  }
  if (!slow) {
  u32 pp[8]; f32x4 pd[8];
  const u32 ptb = l0 + L_PT - 4 * tg, four = 4;
  SLOTS(pd[s] = ldl<f32x4>(mad16hi(rec[s].x, four, ptb));)
  GATHER(5)
  const u32 spare = l0 + L_R + MAXT * 512 + 16 * e32;
  SLOTS(pp[s] = l0 + L_R + 16 * (__float_as_uint(pd[s].w) + rank[s]); pp[s] = V(s) ? pp[s] : spare; stl<u32>(pp[s] + 12, rec[s].x);)
  GATHER(6)
  if (big) GATHER(7)
  {
    const u32 p = wend + lane;
    if (p < 32 * (wtb + wnt)) stl<u32x4>(l0 + L_R + 16 * p, u32x4{0, 0, 0, (4 * NPB + tg) << 16});
  }
  LBAR();
  const u32 slot0 = l0 + L_STG + w * 8192;
  const u32 sraddr = l0 + L_R + 512 * wtb + 16 * (lane >> 3) + 12;
  const u32 c128 = 128;
  auto ldsrc = [&](u32 a) { return u32x4{ldl<u32>(a), ldl<u32>(a + 128), ldl<u32>(a + 256), ldl<u32>(a + 384)}; };
  auto issue = [&](u32 a, u32 b, u32x4 sv, u32 slot) {
    dma4_if(a, b, mad16lo(sv.x, c128, dch0), mad16lo(sv.y, c128, dch1), mad16lo(sv.z, c128, dch0), mad16lo(sv.w, c128, dch1), dh, slot);
  };
  SLOTS(asm volatile("" : "+v"(ps[s].x), "+v"(ps[s].y));)
  issue(0, wnt, ldsrc(sraddr), slot0);
  issue(1, wnt, ldsrc(sraddr + 512), slot0 + 4096);
  SLOTS(const float dx = pd[s].x - lo2f(ps[s].x); const float dy = pd[s].y - hi2f(ps[s].x); const float dz = pd[s].z - lo2f(ps[s].y);
        const float d2 = dx * dx + dy * dy + dz * dz;
        const u32 de_ = __builtin_amdgcn_perm(rec[s].y, cvtpk(d2, 0.f), 0x07060100u);
        stl<u32x2>(pp[s], u32x2{cvtpk(1.f, dx), cvtpk(dy, dz)}); stl<u32>(pp[s] + 8, de_);)
  LBAR();

  u32 sa[4];
#pragma unroll
  for (int s = 0; s < 4; ++s) sa[s] = slot0 + e32 * 128 + 16 * ((2 * s + hh) ^ swz);
  const u32 eq = 4 * hh + ((lane >> 2) & 3), slotA = 4 * ((lane >> 4) & 1) + (lane & 3);
  u32 zv = l0 + L_Z; asm volatile("" : "+v"(zv));
  u32 a_de = l0 + L_R + 512 * wtb + 16 * e32 + 8;
  u32 a_v = l0 + L_R + 512 * wtb + 16 * eq;
  u32 a_sr = sraddr + 1536;
  const u32 ohb = l0 + L_OH + (hh ? 144 : 0), ohl = hh ? 24 : 32, nh8 = (u32)(-32 * hh), dem = hh ? 0xffffffffu : 0u;
  const u32 pA = l0 + L_HM + (8 * hh + r4) * RS + cofs - (RS / 4) * tg;
  const bool spc = hh && r4 >= 2;
  const u32 pB = spc ? l0 + L_WR + (r4 - 2) * 128 + cofs : pA + 4 * RS;
  u32 pM = spc ? 0 : 1; asm volatile("" : "+v"(pM));
  f32x16 Y0, Y1, X0, X1, ZERO;
#pragma unroll
  for (int r = 0; r < 16; ++r) { Y0[r] = 0.f; Y1[r] = 0.f; ZERO[r] = 0.f; }
  u32 base = wa0, tgt = 4 * (wa0 + slotA) + tg;
  int bad = 0;
  const u32 fn = (lane & 31) >> 2, fi = lane & 3;
  u32 f_wx = l0 + L_WX + 16 * hh, f_m = l0 + L_HM + fn * RS + 8 * hh, f_p = l0 + L_PA + 16 * fn + 4 * (fi - 1);
  asm volatile("" : "+v"(f_wx), "+v"(f_m), "+v"(f_p));
  auto flush = [&]() {
    u32x4 c[4];
#pragma unroll
    for (int i = 0; i < 4; ++i) { c[0][i] = cvtpk(Y0[2 * i], Y0[2 * i + 1]); c[1][i] = cvtpk(Y0[8 + 2 * i], Y0[9 + 2 * i]);
                                  c[2][i] = cvtpk(Y1[2 * i], Y1[2 * i + 1]); c[3][i] = cvtpk(Y1[8 + 2 * i], Y1[9 + 2 * i]); }
    f32x16 D = ZERO;
#pragma unroll
    for (int i = 0; i < 4; ++i) D = MFMA(ldl<bf16x8>(f_wx + 32 * i), __builtin_bit_cast(bf16x8, c[i]), D);
    const u32 node = base + fn;
    if (node < wa1) {
      if (fi == 0) {
        const u32 a = f_m + base * RS;
#pragma unroll
        for (int q = 0; q < 4; ++q) {
          stl<u32x2>(a + 16 * q, u32x2{c[q >> 1][2 * (q & 1)], c[q >> 1][2 * (q & 1) + 1]});
          stl<u32x2>(a + 64 + 16 * q, u32x2{c[2 + (q >> 1)][2 * (q & 1)], c[2 + (q >> 1)][2 * (q & 1) + 1]});
        }
      } else if (hh == 0) stl<float>(f_p + 16 * base, D[0] * -LN2);
    }
  };
  if (wnt > 0) {
    bf16x8 As[4], Pb0, Pb1; u32x4 OH; u32x4 P0[2], P1[2];
    u32 lastc, lastn, aoh, aA, aB;
    u32 lastreal;
    lastreal = __builtin_amdgcn_readfirstlane(ldl<u32>(l0 + L_R + 16 * (wend - 1) + 12) >> 16);
    float tt[4];
    auto front = [&](u32 dsb, u32 valid) {
      const u32 first = __builtin_amdgcn_readlane(dsb, 0); lastn = min((u32)__builtin_amdgcn_readlane(dsb, 31), lastreal);
      const u32 wb = min(first, (u32)(4 * (NPB - XW)) + tg);
      bad = max(bad, (int)((lastn - wb) & valid));
      aoh = ohb + 4 * min(dsb - wb + nh8, ohl);
      const u32 wo = wb * (RS / 4);
      aA = pA + wo; aB = umul24(pM, wo) + pB;
    };
    auto rdwin = [&]() { OH = ldl<u32x4>(aoh);
      Pb0 = cat8(ldtr(aA), ldtr(aB)); Pb1 = cat8(ldtr(aA + 64), ldtr(aB + 64)); };
#define EA(X, b) { _Pragma("unroll") for (int k_ = 0; k_ < 4; ++k_) tt[k_] = __builtin_amdgcn_exp2f(X[b + k_]); SB(); \
                   _Pragma("unroll") for (int k_ = 0; k_ < 4; ++k_) tt[k_] = 1.f + tt[k_]; SB(); }
#define RMC(X, b, P) { _Pragma("unroll") for (int k_ = 0; k_ < 4; ++k_) tt[k_] = __builtin_amdgcn_rcpf(tt[k_]); SB(); \
                   _Pragma("unroll") for (int k_ = 0; k_ < 4; ++k_) tt[k_] = vmul(tt[k_], X[b + k_]); SB(); \
                   P[(b) >> 3][((b) >> 1) & 3] = cvtpk(tt[0], tt[1]); P[(b) >> 3][(((b) >> 1) & 3) + 1] = cvtpk(tt[2], tt[3]); SB(); }
#define BF(x) __builtin_bit_cast(bf16x8, x)
    {
      const u32x2 ds2 = ldl<u32x2>(a_de); const u32 de = ds2.x, dsb = ds2.y >> 16; const u32x4 sv = ldsrc(a_sr - 512);
      wait_dma(1, wnt);
#pragma unroll
      for (int s = 0; s < 4; ++s) As[s] = ldl<bf16x8>(sa[s]);
      front(dsb, ~0u); lastc = (lastn - tg) >> 2;
      rdwin();
      asm volatile("s_waitcnt lgkmcnt(0)" ::: "memory");
      issue(2, wnt, sv, slot0);
      OH.w |= de & dem;
      X0 = MFMA(As[0], W[0], ZERO); X0 = MFMA(As[1], W[2], X0); X0 = MFMA(As[2], W[4], X0); X0 = MFMA(As[3], W[6], X0); X0 = MFMA(BF(OH), Pb0, X0);
    }
    const u32 ksw = w >= 4 ? (wnt * 22) >> 5 : 0xffffu;
    if (w >= 4) __builtin_amdgcn_s_setprio(1);
    auto body = [&](auto uc, u32 t) {
      constexpr int U = decltype(uc)::value;
      asm volatile("s_cmp_lg_u32 %0, %1\n\ts_cbranch_scc1 1f\n\ts_setprio 0\n1:" :: "s"(t), "s"(ksw) : "scc");
      const u32x4 dsw = ldsrc(a_v + 12 + 512 * U);
      const u32x2 ds2 = ldl<u32x2>(a_de + 512 * (U + 1)); const u32 de = ds2.x, dsb = ds2.y >> 16;
      const u32x4 sv = ldsrc(a_sr + 512 * U);
      SB();
      X1 = MFMA(BF(OH), Pb1, ZERO); SB();
      EA(X0, 0)
      X1 = MFMA(As[0], W[1], X1); SB();
      RMC(X0, 0, P0)
      X1 = MFMA(As[1], W[3], X1); SB();
      EA(X0, 4)
      X1 = MFMA(As[2], W[5], X1); SB();
      RMC(X0, 4, P0)
      X1 = MFMA(As[3], W[7], X1); SB();
      wait_dma(t + 2, wnt);
#pragma unroll
      for (int s = 0; s < 4; ++s) As[s] = ldl<bf16x8>(sa[s] + 4096 * (U ^ 1));
      SB();
      front(dsb, t + 1 < wnt ? ~0u : 0u);
      u32 sel[4];
#pragma unroll
      for (int a = 0; a < 4; ++a) sel[a] = (dsw[a] >> 16) == tgt ? a_v : zv;
      SB();
      EA(X0, 8) RMC(X0, 8, P0)
      asm volatile("s_waitcnt lgkmcnt(0)" ::: "memory");
      issue(t + 3, wnt, sv, slot0 + 4096 * (U ^ 1));
      rdwin();
      s16x4 Bq[4];
#pragma unroll
      for (int a = 0; a < 4; ++a) Bq[a] = ldtr(sel[a] + 128 * a + 512 * U);
      SB();
      EA(X0, 12) RMC(X0, 12, P0)
      X0 = MFMA(As[0], W[0], ZERO); SB();
      EA(X1, 0)
      X0 = MFMA(As[1], W[2], X0); SB();
      RMC(X1, 0, P1)
      X0 = MFMA(As[2], W[4], X0); SB();
      EA(X1, 4)
      X0 = MFMA(As[3], W[6], X0); SB();
      RMC(X1, 4, P1)
      OH.w |= de & dem; SB();
      X0 = MFMA(BF(OH), Pb0, X0); SB();
      EA(X1, 8)
      bf16x8 B0 = cat8(Bq[0], Bq[1]), B1 = cat8(Bq[2], Bq[3]);
      Y0 = MFMA(BF(P0[0]), B0, Y0); SB();
      RMC(X1, 8, P1)
      Y0 = MFMA(BF(P0[1]), B1, Y0); SB();
      EA(X1, 12)
      Y1 = MFMA(BF(P1[0]), B0, Y1); SB();
      RMC(X1, 12, P1)
      Y1 = MFMA(BF(P1[1]), B1, Y1); SB();
      if (lastc >= base + 8) do {
        flush(); base += 8; tgt += 32;
        asm volatile("" : "+s"(base), "+v"(tgt));
#pragma unroll
        for (int a = 0; a < 4; ++a) Bq[a] = ldtr(((dsw[a] >> 16) == tgt ? a_v : zv) + 128 * a + 512 * U);
        B0 = cat8(Bq[0], Bq[1]); B1 = cat8(Bq[2], Bq[3]);
        Y0 = MFMA(BF(P0[0]), B0, ZERO); Y1 = MFMA(BF(P1[0]), B0, ZERO);
        Y0 = MFMA(BF(P0[1]), B1, Y0); Y1 = MFMA(BF(P1[1]), B1, Y1);
      } while (lastc >= base + 8);
      lastc = (lastn - tg) >> 2;
    };
    for (u32 t = 0;; t += 2) {
      body(IC<0>{}, t);
      if (t + 1 >= wnt) break;
      body(IC<1>{}, t + 1);
      if (t + 2 >= wnt) break;
      a_de += 1024; a_v += 1024; a_sr += 1024;
    }
  }
  {
    dma_rows(slot0);
    u32 sv; const char* g = (const char*)wh + w * 2048; const u32 vo = (lane * 16) ^ ((lane >> 4 & 1) << 6);
    asm volatile("s_mov_b32 %0, m0\n\ts_mov_b32 m0, %3\n\ts_nop 0\n\tglobal_load_lds_dwordx4 %1, %2\n\tglobal_load_lds_dwordx4 %1, %2 offset:1024\n\ts_mov_b32 m0, %0"
                 : "=&s"(sv) : "v"(vo), "s"(g), "s"(slot0 + 4096) : "memory");
  }
  if (base < wa1) { flush(); base += 8; }
  if (base < wa1) {
#pragma unroll
    for (int r = 0; r < 16; ++r) { Y0[r] = 0.f; Y1[r] = 0.f; }
    do { flush(); base += 8; } while (base < wa1);
  }
  if (bad >= 4 * XW && lane == 0) stl<u32>(l0 + L_MISC + 36, 1u);
  asm volatile("s_waitcnt vmcnt(0) lgkmcnt(0)\n\ts_barrier" ::: "memory");
  slow = __builtin_amdgcn_readfirstlane(ldl<u32>(l0 + L_MISC + 36)) != 0;

  if (!slow) {
  if (w < 7) {
    const u32 nl = 32 * w + e32, mrow = l0 + L_HM + min(nl, (u32)NPB) * RS + 16 * hh;
    f32x16 A0, A1, C0, C1;
#pragma unroll
    for (int r = 0; r < 16; ++r) { A0[r] = 0.f; A1[r] = 0.f; C0[r] = 0.f; C1[r] = 0.f; }
#pragma unroll
    for (int s = 0; s < 4; ++s) {
      const bf16x8 hv = ldl<bf16x8>(sa[s]), mb = ldl<bf16x8>(mrow + 32 * s);
      const u32 wa = l0 + L_STG + 4096 + wtr + 64 * (r4 >> 1), wb = l0 + L_STG + 4096 + wtr + 64 - 64 * (r4 >> 1);
      const bf16x8 w0 = cat8(ldtr(wa + s * 8192), ldtr(wa + s * 8192 + 512)), w1 = cat8(ldtr(wb + s * 8192), ldtr(wb + s * 8192 + 512));
      const bf16x8 w2 = cat8(ldtr(wa + (4 + s) * 8192), ldtr(wa + (4 + s) * 8192 + 512)), w3 = cat8(ldtr(wb + (4 + s) * 8192), ldtr(wb + (4 + s) * 8192 + 512));
      A0 = MFMA(w0, hv, A0); A1 = MFMA(w1, hv, A1); C0 = MFMA(w2, mb, C0); C1 = MFMA(w3, mb, C1);
    }
#pragma unroll
    for (int ft = 0; ft < 2; ++ft)
#pragma unroll
      for (int q = 0; q < 4; ++q) {
        const u32 a = slot0 + e32 * 128 + 16 * ((4 * ft + q) ^ swz) + 8 * hh;
        const u32x2 hv = ldl<u32x2>(a);
        float o[4];
#pragma unroll
        for (int k = 0; k < 4; ++k) { const float z = ft ? A1[4 * q + k] - LN2 * C1[4 * q + k] : A0[4 * q + k] - LN2 * C0[4 * q + k]; o[k] = silu_f(z); }
        stl<u32x2>(a, u32x2{cvtpk(lo2f(hv.x) + o[0], hi2f(hv.x) + o[1]), cvtpk(lo2f(hv.y) + o[2], hi2f(hv.y) + o[3])});
      }
    asm volatile("s_waitcnt lgkmcnt(0)" ::: "memory");
#pragma unroll
    for (int a = 0; a < 4; ++a) {
      const u32 row = 8 * a + (lane >> 3), nr = 32 * w + row;
      const u32x4 v = ldl<u32x4>(slot0 + a * 1024 + lane * 16);
      if (nr < NPB && n0 + nr < (u32)N) { u32x4* po_ = (u32x4*)((char*)out + (size_t)(n0 + nr) * 128 + ((a & 1) ? dch1 : dch0)); __builtin_nontemporal_store(v, po_); }
    }
  } else {
    const int nn = min(NPB, N - n0);
    u32* po = (u32*)(out + (size_t)N * 64 + (size_t)n0 * 3);
    for (int i = lane; 2 * i < 3 * nn; i += 64) {
      const int e0 = 2 * i, e1 = e0 + 1, a0 = e0 / 3, a1 = e1 / 3, c0 = e0 - 3 * a0, c1 = e1 - 3 * a1;
      const float v0 = ldl<float>(l0 + L_PT + 16 * a0 + 4 * c0) + ldl<float>(l0 + L_PA + 16 * a0 + 4 * c0);
      const float v1 = ldl<float>(l0 + L_PT + 16 * a1 + 4 * c1) + ldl<float>(l0 + L_PA + 16 * a1 + 4 * c1);
      po[i] = cvtpk(v0, v1);
    }
  }
  }
  }
  if (slow) slow_bucket(h, pos, ea, src, dst, we, wx, wh, out, N, E, bk, l0);
}

__global__ void gen_zero(float* p, int n) { int i = blockIdx.x * blockDim.x + threadIdx.x; if (i < n) p[i] = 0.f; }
__global__ void gen_edge(const u16* h, const u16* pos, const u16* ea, const int* src, const int* dst, const u16* we, const u16* wx, float* mi, float* pd, int E) {
  const int lane = threadIdx.x & 63, w = threadIdx.x >> 6, nw = blockDim.x >> 6;
  const float wxf = bf2f(wx[lane]);
  for (int e = blockIdx.x * nw + w; e < E; e += gridDim.x * nw) {
    const int s = src[e], d = dst[e];
    float df[3], d2 = 0.f;
#pragma unroll
    for (int k = 0; k < 3; ++k) { df[k] = lo2f(cvtpk(bf2f(pos[(size_t)d * 3 + k]) - bf2f(pos[(size_t)s * 3 + k]), 0.f)); d2 += df[k] * df[k]; }
    d2 = lo2f(cvtpk(d2, 0.f));
    const float hd = bf2f(h[(size_t)d * 64 + lane]), hs = bf2f(h[(size_t)s * 64 + lane]);
    float z = 0.f;
#pragma unroll 4
    for (int k = 0; k < 64; ++k) z += __shfl(hd, k) * bf2f(we[k * 64 + lane]) + __shfl(hs, k) * bf2f(we[(64 + k) * 64 + lane]);
    z += d2 * bf2f(we[128 * 64 + lane]) + bf2f(ea[e]) * bf2f(we[129 * 64 + lane]);
    const float m = lo2f(cvtpk(silu_f(z), 0.f));
    float cs = m * wxf;
#pragma unroll
    for (int o = 32; o > 0; o >>= 1) cs += __shfl_xor(cs, o);
    atomicAdd(&mi[(size_t)d * 64 + lane], m);
    if (lane < 3) atomicAdd(&pd[(size_t)d * 3 + lane], (lane == 0 ? df[0] : lane == 1 ? df[1] : df[2]) * cs);
  }
}
__global__ void gen_node(const u16* h, const u16* pos, const u16* wh, const float* mi, const float* pd, u16* out, int N) {
  const int lane = threadIdx.x & 63, w = threadIdx.x >> 6, nw = blockDim.x >> 6;
  for (int n = blockIdx.x * nw + w; n < N; n += gridDim.x * nw) {
    const float hv = bf2f(h[(size_t)n * 64 + lane]), mv = lo2f(cvtpk(mi[(size_t)n * 64 + lane], 0.f));
    float z = 0.f;
#pragma unroll 4
    for (int k = 0; k < 64; ++k) z += __shfl(hv, k) * bf2f(wh[k * 64 + lane]) + __shfl(mv, k) * bf2f(wh[(64 + k) * 64 + lane]);
    out[(size_t)n * 64 + lane] = (u16)cvtpk(hv + silu_f(z), 0.f);
    if (lane < 3) out[(size_t)N * 64 + (size_t)n * 3 + lane] = (u16)cvtpk(bf2f(pos[(size_t)n * 3 + lane]) + pd[(size_t)n * 3 + lane], 0.f);
  }
}

extern "C" void kernel_launch(void* const* d_in, const int* in_sizes, int n_in, void* d_out, int out_size, void* d_ws, size_t ws_size, hipStream_t stream) {
  const u16* h = (const u16*)d_in[0];
  const u16* pos = (const u16*)d_in[1];
  const u16* ea = (const u16*)d_in[2];
  const int* src = (const int*)d_in[3];
  const int* dst = (const int*)d_in[4];
  const u16* we = (const u16*)d_in[5];
  const u16* wx = (const u16*)d_in[6];
  const u16* wh = (const u16*)d_in[7];
  const int N = in_sizes[0] / 64, E = in_sizes[3];
  const int CS = ((E + NCH - 1) / NCH + 3) & ~3;
  const bool fast = N >= 2 && N <= NPB * NBK && (N * 6) % 64 != 0 && (E & 3) == 0 && E > 0 && CS <= 4096 && W_REC + (size_t)NCH * CS * 8 <= ws_size;
  if (fast) {
    (void)hipFuncSetAttribute((const void*)egnn_fused, hipFuncAttributeMaxDynamicSharedMemorySize, L_END);
    egnn_fused<<<NBK, 512, L_END, stream>>>(h, pos, ea, src, dst, we, wx, wh, (char*)d_ws, (u16*)d_out, N, E, CS);
  } else {
    float* mi = (float*)d_ws; float* pd = mi + (size_t)N * 64;
    const int nz = N * 67;
    gen_zero<<<(nz + 255) / 256, 256, 0, stream>>>(mi, nz);
    gen_edge<<<2048, 256, 0, stream>>>(h, pos, ea, src, dst, we, wx, mi, pd, E);
    gen_node<<<1024, 256, 0, stream>>>(h, pos, wh, mi, pd, (u16*)d_out, N);
  }
}
```
